```python
import torch

def _softmax(x, axis=-1):
    return torch.softmax(x, dim=axis)

def _gelu(x):
    return torch.nn.functional.gelu(x, approximate='tanh')

def _silu(x):
    return torch.nn.functional.silu(x)

def _sigmoid(x):
    return torch.sigmoid(x)

def _layernorm(x, axis=-1, eps=1e-05):
    m = x.mean(dim=axis, keepdim=True)
    v = x.var(dim=axis, keepdim=True, unbiased=False)
    return (x - m) / torch.sqrt(v + eps)

def _scatter_add(tgt, idx, src, axis=0):
    return tgt.clone().index_add_(axis, idx, src)

def _segment_sum(src, seg, n):
    out = torch.zeros((n,) + src.shape[1:], dtype=src.dtype)
    return out.index_add_(0, seg, src)

def _pad(x, pw):
    flat = tuple((v for pair in reversed(pw) for v in pair))
    return torch.nn.functional.pad(x, flat)
kernel_name = 'adaLN_cond__memory_aug_recurrent_mem_tr__dense_activation_gelu__prompt'

def setup_inputs():

    _w = lambda a: (lambda t: t.to(torch.bfloat16) if t.is_floating_point() else t)(torch.as_tensor(a))

    def _A_setup():
        B, S, Dm = (1, 2048, 512)
        H, Dh = (32, 128)
        M, seg = (8, 32)
        seg = min(seg, S)
        S = S // seg * seg
        return {'x': _w((lambda *s: torch.randn(*s, dtype=torch.bfloat16) * 0.05)(B, S, Dm)), 'mem0': _w((lambda *s: torch.randn(*s, dtype=torch.bfloat16) * 0.8)(B, M, Dm)), 'Wq': _w((lambda *s: torch.randn(*s, dtype=torch.bfloat16) * 1.6)(Dm, H, Dh) * Dm ** (-0.5)), 'Wk': _w((lambda *s: torch.randn(*s, dtype=torch.bfloat16) * 1.6)(Dm, H, Dh) * Dm ** (-0.5)), 'Wv': _w((lambda *s: torch.randn(*s, dtype=torch.bfloat16) * 0.8)(Dm, H, Dh) * Dm ** (-0.5)), 'Wo': _w((lambda *s: torch.randn(*s, dtype=torch.bfloat16) * 0.8)(H, Dh, Dm) * (H * Dh) ** (-0.5)), 'seg_len': seg}
    _A_in = _A_setup()

    def _B_setup():
        B, S, D, F = (1, 2048, 512, 1376)
        return {'x': _w((lambda *s: torch.randn(*s, dtype=torch.bfloat16) * 0.05)(B, S, D)), 'w1': _w((lambda *s: torch.randn(*s, dtype=torch.bfloat16) * 1.6)(D, F) * D ** (-0.5)), 'b1': _w((lambda *s: torch.randn(*s, dtype=torch.bfloat16) * 1.6)(F) * 0.01), 'w2': _w((lambda *s: torch.randn(*s, dtype=torch.bfloat16) * 0.2)(F, D) * F ** (-0.5)), 'b2': _w((lambda *s: torch.randn(*s, dtype=torch.bfloat16) * 0.2)(D) * 0.01)}
    _B_in = _B_setup()
    return {'A_x': _A_in['x'], 'A_mem0': _A_in['mem0'], 'A_Wq': _A_in['Wq'], 'A_Wk': _A_in['Wk'], 'A_Wv': _A_in['Wv'], 'A_Wo': _A_in['Wo'], 'A_seg_len': _A_in['seg_len'], 'B_x': _B_in['x'], 'B_w1': _B_in['w1'], 'B_b1': _B_in['b1'], 'B_w2': _B_in['w2'], 'B_b2': _B_in['b2'], 'c': _w((lambda *s: torch.randn(*s, dtype=torch.bfloat16) * 0.05)(1, 512) * 8.0), 'w_mod': _w((lambda *s: torch.randn(*s, dtype=torch.bfloat16) * 0.05)(512, 6 * 512) * 512 ** (-0.5) * 8.0)}

def reference(A_x, A_mem0, A_Wq, A_Wk, A_Wv, A_Wo, A_seg_len, B_x, B_w1, B_b1, B_w2, B_b2, c, w_mod):

    def _A_ref(x, mem0, Wq, Wk, Wv, Wo, seg_len):
        B, S, Dm = x.shape
        H, Dh = (Wq.shape[1], Wq.shape[2])
        M = mem0.shape[1]
        scale = Dh ** (-0.5)
        n_seg = S // seg_len
        L = M + seg_len
        idx = torch.arange(L)
        mask = (idx[:, None] < M) | (idx[:, None] >= idx[None, :])
        mem = mem0
        outs = []
        for s in range(n_seg):
            xs = x[:, s * seg_len:(s + 1) * seg_len, :].clone()
            h = torch.concatenate([mem, xs], axis=1)
            q = torch.einsum('btd,dhe->bhte', h, Wq)
            k = torch.einsum('btd,dhe->bhte', h, Wk)
            v = torch.einsum('btd,dhe->bhte', h, Wv)
            scores = torch.einsum('bhqe,bhke->bhqk', q, k) * scale
            scores = torch.where(mask[None, None], scores, -1000000000.0)
            p = _softmax(scores, axis=-1)
            o = torch.einsum('bhqk,bhke->bhqe', p, v)
            o = torch.einsum('bhte,hed->btd', o, Wo)
            mem = o[:, :M, :].clone()
            outs.append(o[:, M:, :])
        return torch.concatenate(outs, axis=1)

    def _B_ref(x, w1, b1, w2, b2):
        h = torch.einsum('bsd,df->bsf', x, w1) + b1
        h = _gelu(h)
        return torch.einsum('bsf,fd->bsd', h, w2) + b2
    x = A_x
    mod = (_silu(c) @ w_mod).reshape(c.shape[0], 6, 1, -1)
    sa, ca, ga, sb, cb, gb = (mod[:, 0].clone(), mod[:, 1].clone(), mod[:, 2].clone(), mod[:, 3].clone(), mod[:, 4].clone(), mod[:, 5].clone())
    h = _layernorm(x, axis=-1) * (1 + ca) + sa
    x = x + ga * _A_ref(h, A_mem0, A_Wq, A_Wk, A_Wv, A_Wo, A_seg_len)
    h = _layernorm(x, axis=-1) * (1 + cb) + sb
    x = x + gb * _B_ref(h, B_w1, B_b1, B_w2, B_b2)
    return x
```

```cpp
#include <hip/hip_runtime.h>
#include <hip/hip_bf16.h>

typedef unsigned u32;
typedef unsigned long long u64;
typedef unsigned short u16;
typedef u32 u32x2 __attribute__((ext_vector_type(2)));
typedef u32 u32x4 __attribute__((ext_vector_type(4)));
typedef u32 u32x8 __attribute__((ext_vector_type(8)));
typedef float f32x4 __attribute__((ext_vector_type(4)));
typedef float f32x2 __attribute__((ext_vector_type(2)));
typedef short s16x4 __attribute__((ext_vector_type(4)));
typedef __bf16 bf16x2 __attribute__((ext_vector_type(2)));
typedef __bf16 bf16x8 __attribute__((ext_vector_type(8)));
typedef __bf16 bf16x16 __attribute__((ext_vector_type(16)));
typedef __amdgpu_buffer_rsrc_t rsrc_t;
#define DEV __device__ __forceinline__

constexpr int NSEG = 64, DM = 512, FF = 1376, NKF = 43;
constexpr u32 OFF_MACC = 0;
constexpr u32 OFF_KVF = 2u << 20;
constexpr u32 OFF_HF = OFF_KVF + 8192;
constexpr u32 OFF_RDY = OFF_HF + 8192;
constexpr u32 OFF_OC = OFF_RDY + 8192;
constexpr u32 OFF_X1C = OFF_OC + 8192;
constexpr u32 OFF_GC = OFF_X1C + 16384;
constexpr u32 ZERO_BYTES = OFF_GC + 16384;
constexpr u32 OFF_MOD = ZERO_BYTES;
constexpr u32 OFF_NRM = OFF_MOD + 12288;
constexpr u32 OFF_H = 3u << 20;
constexpr u32 OFF_KV = 5u << 20;
constexpr u32 OFF_O = 37u << 20;
constexpr u32 OFF_X1 = 53u << 20;
constexpr u32 OFF_G = 55u << 20;
constexpr u32 WS_BYTES = 61u << 20;
constexpr int LDS_BYTES = 147456;
constexpr int SPIN_CAP = 1 << 17;
constexpr int SC1 = 16;
constexpr float SM_C = 0.08838834764831845f * 1.4426950408889634f;

struct Ptrs {
  const u16 *x, *mem0, *wq, *wk, *wv, *wo, *w1, *b1, *w2, *b2;
  u16* out;
  char* ws;
};

DEV float bflo(u32 v) { return __uint_as_float(v << 16); }
DEV float bfhi(u32 v) { return __uint_as_float(v & 0xffff0000u); }
DEV u32 pk(float a, float b) { bf16x2 v = {(__bf16)a, (__bf16)b}; return __builtin_bit_cast(u32, v); }
DEV float rbf(float a) { return (float)(__bf16)a; }
DEV u32 ilo(u32 a, u32 b) { return __builtin_amdgcn_perm(b, a, 0x05040100u); }
DEV u32 ihi(u32 a, u32 b) { return __builtin_amdgcn_perm(b, a, 0x07060302u); }
DEV f32x4 mma(u32x4 a, u32x4 b, f32x4 c) {
  return __builtin_amdgcn_mfma_f32_16x16x32_bf16(__builtin_bit_cast(bf16x8, a), __builtin_bit_cast(bf16x8, b), c, 0, 0, 0);
}
#define MMA_ASM(NAME, OP, TA, CA, CB) \
  DEV void NAME(f32x4& c, TA a, TA b) { asm("s_nop 1\n\t" OP " %0, %1, %2, %0" : "+v"(c) : CA(a), CB(b)); } \
  DEV void NAME##0(f32x4& c, TA a, TA b) { asm("s_nop 1\n\t" OP " %0, %1, %2, 0" : "=&v"(c) : CA(a), CB(b)); }
MMA_ASM(mvv, "v_mfma_f32_16x16x32_bf16", u32x4, "v", "v")
MMA_ASM(mav, "v_mfma_f32_16x16x32_bf16", u32x4, "a", "v")
MMA_ASM(mva, "v_mfma_f32_16x16x32_bf16", u32x4, "v", "a")
MMA_ASM(m16, "v_mfma_f32_16x16x16_bf16", u32x2, "v", "v")
DEV void fin(f32x4& a) { asm volatile("s_nop 7" : "+v"(a)); }
DEV void fin(f32x4& a, f32x4& b) { asm volatile("s_nop 7" : "+v"(a), "+v"(b)); }
DEV void fin(f32x4& a, f32x4& b, f32x4& c) { asm volatile("s_nop 7" : "+v"(a), "+v"(b), "+v"(c)); }
DEV void fin(f32x4& a, f32x4& b, f32x4& c, f32x4& d) { asm volatile("s_nop 7" : "+v"(a), "+v"(b), "+v"(c), "+v"(d)); }
typedef __attribute__((address_space(3))) char* lptr;
DEV lptr lfix(char* p) { lptr q = (lptr)p; asm volatile("" : "+v"(q)); return q; }
#define LRD(p) (*(const __attribute__((address_space(3))) u32x4*)(p))
#define LWR(p) (*(__attribute__((address_space(3))) u32x4*)(p))
#define LW1(p) (*(__attribute__((address_space(3))) u32*)(p))
DEV f32x4 smma(u32x4 a, u32x8 b, f32x4 c, int idx) {
  return __builtin_amdgcn_smfmac_f32_16x16x64_bf16(__builtin_bit_cast(bf16x8, a), __builtin_bit_cast(bf16x16, b), c, idx, 0, 0);
}
DEV float swapadd(float a, float b) {
  auto t = __builtin_amdgcn_permlane32_swap(__float_as_uint(a), __float_as_uint(b), false, false);
  return __uint_as_float(t[0]) + __uint_as_float(t[1]);
}
DEV float xmax(float x) {
  auto s = __builtin_amdgcn_permlane16_swap(__float_as_uint(x), __float_as_uint(x), false, false);
  x = fmaxf(__uint_as_float(s[0]), __uint_as_float(s[1]));
  auto t = __builtin_amdgcn_permlane32_swap(__float_as_uint(x), __float_as_uint(x), false, false);
  return fmaxf(__uint_as_float(t[0]), __uint_as_float(t[1]));
}
DEV float xsum(float x) {
  auto s = __builtin_amdgcn_permlane16_swap(__float_as_uint(x), __float_as_uint(x), false, false);
  x = __uint_as_float(s[0]) + __uint_as_float(s[1]);
  auto t = __builtin_amdgcn_permlane32_swap(__float_as_uint(x), __float_as_uint(x), false, false);
  return __uint_as_float(t[0]) + __uint_as_float(t[1]);
}
template <int C> DEV float dpp(float x) { return __uint_as_float(__builtin_amdgcn_update_dpp(0, __float_as_uint(x), C, 0xf, 0xf, true)); }
DEV float sum8(float x) { x += dpp<0xB1>(x); x += dpp<0x4E>(x); return x + dpp<0x141>(x); }
DEV float sum16(float x) { x = sum8(x); return x + dpp<0x128>(x); }
DEV float max16(float x) { x = fmaxf(x, dpp<0xB1>(x)); x = fmaxf(x, dpp<0x4E>(x)); x = fmaxf(x, dpp<0x141>(x)); return fmaxf(x, dpp<0x128>(x)); }
DEV void lds_bar() { asm volatile("s_waitcnt lgkmcnt(0)\n\ts_barrier" ::: "memory"); }
DEV void drain() { asm volatile("s_waitcnt vmcnt(0)" ::: "memory"); }
DEV u32x4 ld4(rsrc_t r, u32 off) { return __builtin_amdgcn_raw_buffer_load_b128(r, off, 0, SC1); }
DEV void st4(rsrc_t r, u32 off, u32x4 v) { __builtin_amdgcn_raw_buffer_store_b128(v, r, off, 0, SC1); }
DEV u32 ldw(const char* ws, u32 off) { return __hip_atomic_load((const u32*)(ws + off), __ATOMIC_RELAXED, __HIP_MEMORY_SCOPE_AGENT); }
DEV void stw(char* ws, u32 off, u32 v) { __hip_atomic_store((u32*)(ws + off), v, __ATOMIC_RELAXED, __HIP_MEMORY_SCOPE_AGENT); }
DEV void addw(char* ws, u32 off) { __hip_atomic_fetch_add((u32*)(ws + off), 1u, __ATOMIC_RELAXED, __HIP_MEMORY_SCOPE_AGENT); }
template <int SLEEP> DEV u32 wait_ge(const char* ws, u32 off, u32 target) {
  u32 v = 0;
  for (int n = 0; n < SPIN_CAP; ++n) {
    v = __builtin_amdgcn_readfirstlane(ldw(ws, off));
    if (v >= target) break;
    __builtin_amdgcn_s_sleep(SLEEP);
  }
  asm volatile("" ::: "memory");
  return v;
}
DEV f32x4 zv() { f32x4 z = {0, 0, 0, 0}; asm volatile("" : "+v"(z)); return z; }
template <class T> DEV void pin_acc(T& v) { asm volatile("" : "+a"(v)); }
template <int NK, bool ACC> DEV void load_w2(u32x4 (&w)[NK][2], const u16* base, u32 kstep, u32 rstride, u32 loff) {
#pragma unroll
  for (int ks = 0; ks < NK; ++ks) {
    u32 t[8];
#pragma unroll
    for (int j = 0; j < 8; ++j) {
      t[j] = *(const u32*)(base + (size_t)(ks * kstep + j * rstride) + loff);
    }
#pragma unroll
    for (int p = 0; p < 4; ++p) { w[ks][0][p] = ilo(t[2 * p], t[2 * p + 1]); w[ks][1][p] = ihi(t[2 * p], t[2 * p + 1]); }
    if (ACC) { pin_acc(w[ks][0]); pin_acc(w[ks][1]); }
  }
}
DEV u32 dec(u32 lo, u32 hi, float inv) { return pk(fmaf((float)(int)lo, inv, 976.f * inv), (float)(int)hi * inv); }
DEV void poll_macc(rsrc_t r, u32 off, u32x4 (&q)[4]) {
  for (int n = 0; n < SPIN_CAP; ++n) {
    asm volatile("" ::: "memory");
#pragma unroll
    for (int i = 0; i < 4; ++i) q[i] = ld4(r, off + i * 2048);
    u32 m = 32;
#pragma unroll
    for (int i = 0; i < 4; ++i) m &= q[i][0] & q[i][2];
    if (__all(m != 0)) break;
  }
}

__global__ __launch_bounds__(256) void setup_kernel(const u16* c, const u16* wmod, const u16* wv, const u16* wo, char* ws) {
  __shared__ float red[32 * 64];
  __shared__ float sc[512];
  const int b = blockIdx.x, tid = threadIdx.x;
  for (u32 i = b * 256 + tid; i < ZERO_BYTES / 16; i += gridDim.x * 256) ((u32x4*)ws)[i] = u32x4{0, 0, 0, 0};
  float* mod = (float*)(ws + OFF_MOD);
  float* nrm = (float*)(ws + OFF_NRM);
  if (b < 48) {
    u32x4 wr[16];
#pragma unroll
    for (int it = 0; it < 16; ++it) wr[it] = *(const u32x4*)(wmod + (size_t)(it * 32 + (tid >> 3)) * 3072 + b * 64 + (tid & 7) * 8);
    for (int i = tid; i < 512; i += 256) { float v = bflo(c[i]); sc[i] = rbf(v / (1.f + __expf(-v))); }
    __syncthreads();
    float acc[8] = {0, 0, 0, 0, 0, 0, 0, 0};
#pragma unroll
    for (int it = 0; it < 16; ++it) {
      float s = sc[it * 32 + (tid >> 3)];
#pragma unroll
      for (int p = 0; p < 4; ++p) { acc[2 * p] += s * bflo(wr[it][p]); acc[2 * p + 1] += s * bfhi(wr[it][p]); }
    }
#pragma unroll
    for (int j = 0; j < 8; ++j) red[(tid >> 3) * 64 + (tid & 7) * 8 + j] = acc[j];
    __syncthreads();
    if (tid < 64) { float s = 0; for (int i = 0; i < 32; ++i) s += red[i * 64 + tid]; mod[b * 64 + tid] = rbf(s); }
  } else {
    const int h = (b - 48) >> 3, part = (b - 48) & 7;
    u32x4 v[4], o[4];
#pragma unroll
    for (int i = 0; i < 4; ++i) {
      v[i] = *(const u32x4*)(wv + (size_t)(part * 64 + i * 16 + (tid >> 4)) * 4096 + h * 128 + (tid & 15) * 8);
      o[i] = *(const u32x4*)(wo + (size_t)(h * 128 + i * 32 + (tid >> 3)) * 512 + part * 64 + (tid & 7) * 8);
    }
    float a = 0, cs[8] = {0, 0, 0, 0, 0, 0, 0, 0};
#pragma unroll
    for (int i = 0; i < 4; ++i)
#pragma unroll
      for (int p = 0; p < 4; ++p) {
        a += bflo(v[i][p]) * bflo(v[i][p]) + bfhi(v[i][p]) * bfhi(v[i][p]);
        cs[2 * p] += bflo(o[i][p]) * bflo(o[i][p]); cs[2 * p + 1] += bfhi(o[i][p]) * bfhi(o[i][p]);
      }
#pragma unroll
    for (int j = 0; j < 8; ++j) red[(tid >> 3) * 64 + (tid & 7) * 8 + j] = cs[j];
    sc[tid] = a;
    __syncthreads();
    if (tid < 64) {
      float s = 0; for (int i = 0; i < 32; ++i) s += red[i * 64 + tid];
      float t = sc[tid] + sc[tid + 64] + sc[tid + 128] + sc[tid + 192];
      s = xmax(max16(s)); t = xsum(sum16(t));
      if (tid == 0) { nrm[h * 8 + part] = t; nrm[256 + h * 8 + part] = s; }
    }
  }
}

DEV void chain_role(const Ptrs& P, char* lds, rsrc_t rw, int h, int lane, int w) {
  char* L_WV = lds + w * 32768;
  char* L_M = lds + 131072;
  char* L_Q = L_M + 8192;
  char* L_K = L_Q + 2304;
  char* L_O = L_K + 2304;
  const int c = lane & 15, g = lane >> 4;
  const int sidx = (lane & 8) ? 0xEEEE : 0x4444;
  const float* mod = (const float*)(P.ws + OFF_MOD);
  const float* nrm = (const float*)(P.ws + OFF_NRM);
  u32x8 wqk[4][8], wo[8][2];
  {
    const u16* wb = (w < 2 ? P.wq : P.wk) + h * 128 + (w & 1) * 64;
    const u32 loff = (16 * (g >> 1) + 2 * (g & 1)) * 4096;
#pragma unroll
    for (int s = 0; s < 8; ++s) {
#pragma unroll
      for (int p = 0; p < 8; ++p) {
        const int row = 128 * (s >> 1) + 4 * (s & 1) + 64 * (p >> 2) + 16 * (p & 1) + 8 * ((p >> 1) & 1);
        const u16* a = wb + (size_t)row * 4096 + (2 * loff - 2 * (g & 1) * 4096) + 4 * c;
        u32x2 r0 = *(const u32x2*)a, r1 = *(const u32x2*)(a + 4096);
        wqk[0][s][p] = ilo(r0[0], r1[0]); wqk[1][s][p] = ihi(r0[0], r1[0]);
        wqk[2][s][p] = ilo(r0[1], r1[1]); wqk[3][s][p] = ihi(r0[1], r1[1]);
      }
#pragma unroll
      for (int t = 0; t < 4; ++t) pin_acc(wqk[t][s]);
    }
    const u16* vb = P.wv + h * 128 + 32 * w + 2 * c;
#pragma unroll
    for (int s = 0; s < 8; ++s) {
      u32x8 f0, f1;
#pragma unroll
      for (int p = 0; p < 8; ++p) {
        const int row = 128 * (s >> 1) + 4 * (s & 1) + 64 * (p >> 2) + 16 * (p & 1) + 8 * ((p >> 1) & 1);
        const u16* a = vb + (size_t)row * 4096 + (2 * loff - 2 * (g & 1) * 4096);
        u32 r0 = *(const u32*)a, r1 = *(const u32*)(a + 4096);
        f0[p] = ilo(r0, r1); f1[p] = ihi(r0, r1);
      }
      *(u32x4*)(L_WV + ((s * 2 + 0) * 64 + lane) * 16) = f0.lo; *(u32x4*)(L_WV + ((s * 2 + 1) * 64 + lane) * 16) = f0.hi;
      *(u32x4*)(L_WV + ((16 + s * 2 + 0) * 64 + lane) * 16) = f1.lo; *(u32x4*)(L_WV + ((16 + s * 2 + 1) * 64 + lane) * 16) = f1.hi;
    }
    const u16* ob = P.wo + (size_t)(h * 128 + 16 * (g >> 1) + 4 * (g & 1)) * 512 + 128 * w + 8 * c;
#pragma unroll
    for (int s = 0; s < 2; ++s)
#pragma unroll
      for (int p = 0; p < 8; ++p) {
        const int row = 64 * s + 32 * (p >> 2) + 8 * (p & 1) + 2 * ((p >> 1) & 1);
        u32x4 r0 = *(const u32x4*)(ob + row * 512), r1 = *(const u32x4*)(ob + row * 512 + 512);
#pragma unroll
        for (int u = 0; u < 4; ++u) { wo[2 * u][s][p] = ilo(r0[u], r1[u]); wo[2 * u + 1][s][p] = ihi(r0[u], r1[u]); }
      }
  }
  float gb, hx2;
  {
    float a = 0, b = 0;
#pragma unroll
    for (int i = 0; i < 8; ++i) { a += nrm[(lane & 31) * 8 + i]; b = fmaxf(b, nrm[256 + (lane & 31) * 8 + i]); }
    gb = 0.5f * 1.03f * xsum(sum16(sqrtf(a * b)));
    float s2 = 0, cm = 0;
#pragma unroll
    for (int i = 0; i < 8; ++i) { float sa = mod[lane * 8 + i]; s2 += sa * sa; cm = fmaxf(cm, fabsf(1.f + mod[512 + lane * 8 + i])); }
    float hx = 1.01f * (22.6275f * xmax(max16(cm)) + sqrtf(xsum(sum16(s2))));
    hx2 = hx * hx;
  }
  const int mtok = lane >> 3, msub = lane & 7;
  const lptr mw = lfix(L_M + (2 * w * 64 + msub * 8 + (mtok ^ msub)) * 16);
  const lptr mr = lfix(L_M + ((lane >> 3) * 8 + ((lane & 7) ^ (lane >> 3))) * 16);
  {
    const u16* a = P.mem0 + mtok * 512 + 128 * w + 16 * msub;
#pragma unroll
    for (int f = 0; f < 2; ++f) {
      u32x2 x0 = *(const u32x2*)(a + 4 * f), x1 = *(const u32x2*)(a + 4 * f + 8);
      LWR(mw + f * 1024) = u32x4{x0[0], x1[0], x0[1], x1[1]};
    }
  }
  const u32 kvf = OFF_KVF + h * 256;
  u32x4 kx[8], vx[2];
  wait_ge<2>(P.ws, kvf, 1);
#pragma unroll
  for (int i = 0; i < 8; ++i) kx[i] = ld4(rw, OFF_KV + h * 16384 + i * 1024 + lane * 16);
#pragma unroll
  for (int a = 0; a < 2; ++a) vx[a] = ld4(rw, OFF_KV + h * 16384 + 8192 + (2 * w + a) * 1024 + lane * 16);
  u32 fl = ldw(P.ws, kvf + 4);
  const lptr qkw = lfix((w < 2 ? L_Q : L_K) + (8 * (w & 1) + (c >> 1)) * 144 + 64 * (g & 1) + 4 * (c & 1) + 8 * (lane >> 5));
  const lptr qkr = lfix(L_Q + g * 144 + (lane & 7) * 16);
  const lptr wvr = lfix(L_WV + lane * 16);
  const lptr ow = lfix(L_O + ((4 * w + g) * 8 + (lane & 7)) * 16), orr = lfix(L_O + lane * 16);
  const u32 aoff = (w * 32 + (lane >> 5) * 8 + 4 * (g & 1)) * 256 + c * 8;
  const u32 poff = (w * 32 + mtok) * 256 + msub * 16;
  lds_bar();

  for (int s = 0; s < NSEG - 1; ++s) {
    f32x4 aq[4] = {zv(), zv(), zv(), zv()};
    {
      u32x4 mf[8];
#pragma unroll
      for (int k = 0; k < 8; ++k) mf[k] = LRD(mr + k * 1024);
#pragma unroll
      for (int k = 0; k < 8; ++k)
#pragma unroll
        for (int t = 0; t < 4; ++t) aq[t] = smma(mf[k], wqk[t][k], aq[t], sidx);
    }
    asm volatile("" ::: "memory");
#pragma unroll
    for (int r = 0; r < 4; ++r) LW1(qkw + r * 16) = pk(swapadd(aq[0][r], aq[1][r]), swapadd(aq[2][r], aq[3][r]));
    f32x4 av[2] = {zv(), zv()}, gm, sm, s0, s1;
#pragma unroll
    for (int k = 0; k < 8; ++k) {
      if (k == 4) {
        lds_bar();
#pragma unroll
        for (int e = 0; e < 4; ++e) {
          u32x4 qf = LRD(qkr + e * 576), kf = LRD(qkr + 2304 + e * 576);
          if (e == 0) { mvv0(s0, kx[0], qf); mvv0(s1, kx[4], qf); mvv0(sm, kf, qf); }
          else { mvv(s0, kx[e], qf); mvv(s1, kx[4 + e], qf); mvv(sm, kf, qf); }
        }
      }
      const u32x4 mf = LRD(mr + k * 1024);
#pragma unroll
      for (int a = 0; a < 2; ++a) {
        u32x8 b; b.lo = LRD(wvr + (a * 16 + k * 2) * 1024); b.hi = LRD(wvr + (a * 16 + k * 2 + 1) * 1024);
        av[a] = smma(mf, b, av[a], sidx);
      }
      if (k == 0) mvv0(gm, mf, mf); else mvv(gm, mf, mf);
    }
    fin(sm, s0, s1);
    float mx = fmaxf(fmaxf(fmaxf(sm[0], sm[1]), fmaxf(sm[2], sm[3])), fmaxf(fmaxf(fmaxf(s0[0], s0[1]), fmaxf(s0[2], s0[3])), fmaxf(fmaxf(s1[0], s1[1]), fmaxf(s1[2], s1[3]))));
    mx = xmax(mx) * SM_C;
#pragma unroll
    for (int r = 0; r < 4; ++r) {
      sm[r] = __builtin_amdgcn_exp2f(fmaf(sm[r], SM_C, -mx)); s0[r] = __builtin_amdgcn_exp2f(fmaf(s0[r], SM_C, -mx)); s1[r] = __builtin_amdgcn_exp2f(fmaf(s1[r], SM_C, -mx));
    }
    float rs = __builtin_amdgcn_rcpf(xsum(0.5f * (sm[0] + sm[1] + sm[2] + sm[3]) + (s0[0] + s0[1] + s0[2] + s0[3]) + (s1[0] + s1[1] + s1[2] + s1[3])));
    u32x2 pm = {pk(sm[0] * rs, sm[1] * rs), pk(sm[2] * rs, sm[3] * rs)};
    u32x4 px = {pk(s0[0] * rs, s0[1] * rs), pk(s0[2] * rs, s0[3] * rs), pk(s1[0] * rs, s1[1] * rs), pk(s1[2] * rs, s1[3] * rs)};
    fin(gm);
    float n2 = 2.f * xmax(max16(fmaxf(fmaxf(gm[0], gm[1]), fmaxf(gm[2], gm[3]))));
    const float bound = fmaxf(sqrtf(fmaxf(n2, hx2)) * gb, 1e-20f);
    const float scale = 2080374784.f / bound, inv = bound * (1.f / 2080374784.f);
    f32x4 o[2];
#pragma unroll
    for (int a = 0; a < 2; ++a) {
      mvv0(o[a], vx[a], px);
      m16(o[a], u32x2{pk(av[a][0], av[a][1]), pk(av[a][2], av[a][3])}, pm);
    }
    fin(o[0], o[1]);
    if (!(lane & 8))
      LWR(ow) =
          u32x4{pk(o[0][0] * scale, o[1][0] * scale), pk(o[0][1] * scale, o[1][1] * scale), pk(o[0][2] * scale, o[1][2] * scale), pk(o[0][3] * scale, o[1][3] * scale)};
    lds_bar();
    u32x4 of0 = LRD(orr), of1 = LRD(orr + 1024);
    if (s + 1 < NSEG - 1) {
      if (__builtin_amdgcn_readfirstlane(fl) == 0) wait_ge<2>(P.ws, kvf + 4 * (s + 1), 1);
      const u32 kb = OFF_KV + ((s + 1) * 32 + h) * 16384 + lane * 16;
#pragma unroll
      for (int i = 0; i < 8; ++i) kx[i] = ld4(rw, kb + i * 1024);
#pragma unroll
      for (int a = 0; a < 2; ++a) vx[a] = ld4(rw, kb + 8192 + (2 * w + a) * 1024);
      fl = ldw(P.ws, kvf + 4 * (s + 2));
    }
    char* ab = P.ws + OFF_MACC + (s + 1) * 32768;
#pragma unroll
    for (int q = 0; q < 2; ++q) {
      f32x4 t[4];
#pragma unroll
      for (int i = 0; i < 4; ++i) { t[i] = smma(of0, wo[4 * q + i][0], zv(), sidx); t[i] = smma(of1, wo[4 * q + i][1], t[i], sidx); }
#pragma unroll
      for (int r = 0; r < 4; ++r) {
        u32 lo = ((u32)(int)swapadd(t[0][r], t[2][r]) & ~63u) | 1u, hi = (u32)(int)swapadd(t[1][r], t[3][r]);
        __hip_atomic_fetch_add((u64*)(ab + aoff + (q * 16 + r) * 256), ((u64)hi << 32) | lo, __ATOMIC_RELAXED, __HIP_MEMORY_SCOPE_AGENT);
      }
    }
    drain();
    u32x4 q[4];
    poll_macc(rw, OFF_MACC + (s + 1) * 32768 + poff, q);
#pragma unroll
    for (int f = 0; f < 2; ++f)
      LWR(mw + f * 1024) = u32x4{dec(q[2 * f][0], q[2 * f][1], inv), dec(q[2 * f][2], q[2 * f][3], inv), dec(q[2 * f + 1][0], q[2 * f + 1][1], inv), dec(q[2 * f + 1][2], q[2 * f + 1][3], inv)};
    lds_bar();
    if (h == 0 && w == 0 && lane == 0) stw(P.ws, OFF_RDY + (s + 1) * 128, __float_as_uint(inv));
  }
}

DEV void proj_role(const Ptrs& P, char* lds, rsrc_t rw, int h, int r0, int tid, int lane, int w) {
  char* L_H = lds;
  char* L_S = lds + 32768;
  char* L_MT = lds + 81920;
  char* L_KM = lds + 98304;
  const int c = lane & 15, g = lane >> 4;
  u32x4 wq[16][2], wk[16][2], wv[16][2];
  const u32 loff = g * 8 * 4096 + h * 128 + 32 * w + 2 * c;
  load_w2<16, true>(wk, P.wk, 32 * 4096, 4096, loff);
  load_w2<16, true>(wv, P.wv, 32 * 4096, 4096, loff);
  load_w2<16, false>(wq, P.wq, 32 * 4096, 4096, loff);
#pragma unroll
  for (int i = 0; i < 4; ++i) *(u32x4*)(L_MT + (i * 256 + tid) * 16) = u32x4{0, 0, 0, 0};
  for (int i = 0; i <= 16; ++i) {
    if (i < 16) {
      const int s = 4 * i + r0;
      char* slot = L_S + (i & 1) * 24576;
      wait_ge<8>(P.ws, OFF_HF + s * 128, 1);
      u32x4 t[8];
#pragma unroll
      for (int j = 0; j < 8; ++j) t[j] = ld4(rw, OFF_H + s * 32768 + (j * 256 + tid) * 16);
#pragma unroll
      for (int j = 0; j < 8; ++j) *(u32x4*)(L_H + (j * 256 + tid) * 16) = t[j];
      lds_bar();
      const u32 kb = OFF_KV + (s * 32 + h) * 16384 + lane * 16;
      u32x2 vp[2][2];
#pragma unroll
      for (int mt = 0; mt < 2; ++mt) {
        f32x4 ak[2], av[2];
#pragma unroll
        for (int ks = 0; ks < 16; ++ks) {
          u32x4 hf = *(const u32x4*)(L_H + ((mt * 16 + ks) * 64 + lane) * 16);
#pragma unroll
          for (int a = 0; a < 2; ++a) {
            if (ks == 0) { mav0(ak[a], wk[ks][a], hf); mva0(av[a], hf, wv[ks][a]); } else { mav(ak[a], wk[ks][a], hf); mva(av[a], hf, wv[ks][a]); }
          }
        }
        fin(ak[0], ak[1], av[0], av[1]);
        u32x4 kf = {pk(ak[0][0], ak[0][1]), pk(ak[0][2], ak[0][3]), pk(ak[1][0], ak[1][1]), pk(ak[1][2], ak[1][3])};
        st4(rw, kb + (mt * 4 + w) * 1024, kf);
        *(u32x4*)(slot + 8192 + ((mt * 4 + w) * 64 + lane) * 16) = kf;
#pragma unroll
        for (int a = 0; a < 2; ++a) vp[mt][a] = u32x2{pk(av[a][0], av[a][1]), pk(av[a][2], av[a][3])};
      }
#pragma unroll
      for (int a = 0; a < 2; ++a) {
        u32x4 vf = {vp[0][a][0], vp[0][a][1], vp[1][a][0], vp[1][a][1]};
        st4(rw, kb + 8192 + (2 * w + a) * 1024, vf);
        *(u32x4*)(slot + 16384 + ((2 * w + a) * 64 + lane) * 16) = vf;
      }
#pragma unroll
      for (int mt = 0; mt < 2; ++mt) {
        f32x4 aq[2];
#pragma unroll
        for (int ks = 0; ks < 16; ++ks) {
          u32x4 hf = *(const u32x4*)(L_H + ((mt * 16 + ks) * 64 + lane) * 16);
#pragma unroll
          for (int a = 0; a < 2; ++a) { if (ks == 0) mvv0(aq[a], wq[ks][a], hf); else mvv(aq[a], wq[ks][a], hf); }
        }
        fin(aq[0], aq[1]);
        *(u32x4*)(slot + ((mt * 4 + w) * 64 + lane) * 16) = u32x4{pk(aq[0][0], aq[0][1]), pk(aq[0][2], aq[0][3]), pk(aq[1][0], aq[1][1]), pk(aq[1][2], aq[1][3])};
      }
      drain();
      lds_bar();
      if (tid == 0) stw(P.ws, OFF_KVF + h * 256 + s * 4, 1u);
    }
    if (i >= 1) {
      const int s = 4 * (i - 1) + r0;
      const char* slot = L_S + ((i - 1) & 1) * 24576 + lane * 16;
      f32x4 x00, x01, x11;
#pragma unroll
      for (int k = 0; k < 4; ++k) {
        u32x4 q0 = *(const u32x4*)(slot + k * 1024), q1 = *(const u32x4*)(slot + (4 + k) * 1024);
        u32x4 k0 = *(const u32x4*)(slot + (8 + k) * 1024), k1 = *(const u32x4*)(slot + (12 + k) * 1024);
        if (k == 0) { mvv0(x00, k0, q0); mvv0(x01, k0, q1); mvv0(x11, k1, q1); } else { mvv(x00, k0, q0); mvv(x01, k0, q1); mvv(x11, k1, q1); }
      }
      fin(x00, x01, x11);
      const int tok = lane >> 3, sub = lane & 7;
      if (s == 0) {
#pragma unroll
        for (int j = 0; j < 2; ++j) {
          const int u = j * 256 + tid, t = u >> 6, cg = u & 63;
          *(u32x4*)(L_MT + ((cg >> 2) * 64 + t + 16 * (cg & 3)) * 16) = *(const u32x4*)(P.mem0 + t * 512 + cg * 8);
        }
      } else {
        const float inv = __uint_as_float(wait_ge<4>(P.ws, OFF_RDY + s * 128, 1));
        u32x4 q[4];
        poll_macc(rw, OFF_MACC + s * 32768 + (w * 32 + tok) * 256 + sub * 16, q);
#pragma unroll
        for (int u = 0; u < 4; ++u)
#pragma unroll
          for (int e = 0; e < 2; ++e)
            *(u32*)(L_MT + ((4 * w + (sub >> 1)) * 64 + tok + 16 * ((2 * sub + e) & 3)) * 16 + 4 * u) = dec(q[u][2 * e], q[u][2 * e + 1], inv);
      }
      lds_bar();
      f32x4 ak[2], av[2];
#pragma unroll
      for (int ks = 0; ks < 16; ++ks) {
        u32x4 mf = *(const u32x4*)(L_MT + (ks * 64 + lane) * 16);
#pragma unroll
        for (int a = 0; a < 2; ++a) {
          if (ks == 0) { mav0(ak[a], wk[ks][a], mf); mva0(av[a], mf, wv[ks][a]); } else { mav(ak[a], wk[ks][a], mf); mva(av[a], mf, wv[ks][a]); }
        }
      }
      fin(ak[0], ak[1], av[0], av[1]);
      *(u32x4*)(L_KM + (w * 64 + lane) * 16) = u32x4{pk(ak[0][0], ak[0][1]), pk(ak[0][2], ak[0][3]), pk(ak[1][0], ak[1][1]), pk(ak[1][2], ak[1][3])};
      lds_bar();
      f32x4 m0, m1;
#pragma unroll
      for (int k = 0; k < 4; ++k) {
        u32x4 km = *(const u32x4*)(L_KM + (k * 64 + lane) * 16);
        u32x4 q0 = *(const u32x4*)(slot + k * 1024), q1 = *(const u32x4*)(slot + (4 + k) * 1024);
        if (k == 0) { mvv0(m0, km, q0); mvv0(m1, km, q1); } else { mvv(m0, km, q0); mvv(m1, km, q1); }
      }
      fin(m0, m1);
      const float NEG = -__builtin_inff();
#pragma unroll
      for (int r = 0; r < 4; ++r) {
        if (g >= 2) { m0[r] = NEG; m1[r] = NEG; }
        if (4 * g + r > c) { x00[r] = NEG; x11[r] = NEG; }
      }
      float a0 = xmax(fmaxf(fmaxf(fmaxf(m0[0], m0[1]), fmaxf(m0[2], m0[3])), fmaxf(fmaxf(x00[0], x00[1]), fmaxf(x00[2], x00[3])))) * SM_C;
      float a1 = xmax(fmaxf(fmaxf(fmaxf(fmaxf(m1[0], m1[1]), fmaxf(m1[2], m1[3])), fmaxf(fmaxf(x01[0], x01[1]), fmaxf(x01[2], x01[3]))),
                            fmaxf(fmaxf(x11[0], x11[1]), fmaxf(x11[2], x11[3])))) * SM_C;
      float t0 = 0, t1 = 0;
#pragma unroll
      for (int r = 0; r < 4; ++r) {
        m0[r] = __builtin_amdgcn_exp2f(fmaf(m0[r], SM_C, -a0)); x00[r] = __builtin_amdgcn_exp2f(fmaf(x00[r], SM_C, -a0));
        m1[r] = __builtin_amdgcn_exp2f(fmaf(m1[r], SM_C, -a1)); x01[r] = __builtin_amdgcn_exp2f(fmaf(x01[r], SM_C, -a1)); x11[r] = __builtin_amdgcn_exp2f(fmaf(x11[r], SM_C, -a1));
        t0 += m0[r] + x00[r]; t1 += m1[r] + x01[r] + x11[r];
      }
      t0 = __builtin_amdgcn_rcpf(xsum(t0)); t1 = __builtin_amdgcn_rcpf(xsum(t1));
      u32x2 pm0 = {pk(m0[0] * t0, m0[1] * t0), pk(m0[2] * t0, m0[3] * t0)}, pm1 = {pk(m1[0] * t1, m1[1] * t1), pk(m1[2] * t1, m1[3] * t1)};
      u32x4 px0 = {pk(x00[0] * t0, x00[1] * t0), pk(x00[2] * t0, x00[3] * t0), 0u, 0u};
      u32x4 px1 = {pk(x01[0] * t1, x01[1] * t1), pk(x01[2] * t1, x01[3] * t1), pk(x11[0] * t1, x11[1] * t1), pk(x11[2] * t1, x11[3] * t1)};
      f32x4 o0[2], o1[2];
#pragma unroll
      for (int a = 0; a < 2; ++a) {
        u32x4 vf = *(const u32x4*)(slot + (16 + 2 * w + a) * 1024);
        u32x2 vm = {pk(av[a][0], av[a][1]), pk(av[a][2], av[a][3])};
        mvv0(o0[a], vf, px0); m16(o0[a], vm, pm0);
        mvv0(o1[a], vf, px1); m16(o1[a], vm, pm1);
      }
      fin(o0[0], o0[1], o1[0], o1[1]);
      const u32 ob = OFF_O + (s * 256 + 4 * h + w) * 1024 + lane * 16;
      st4(rw, ob, u32x4{pk(o0[0][0], o0[1][0]), pk(o0[0][1], o0[1][1]), pk(o0[0][2], o0[1][2]), pk(o0[0][3], o0[1][3])});
      st4(rw, ob + 131072, u32x4{pk(o1[0][0], o1[1][0]), pk(o1[0][1], o1[1][1]), pk(o1[0][2], o1[1][2]), pk(o1[0][3], o1[1][3])});
      drain();
      lds_bar();
      if (tid == 0) addw(P.ws, OFF_OC + s * 128);
    }
  }
}

DEV void oproj_role(const Ptrs& P, char* lds, rsrc_t rw, int n, int th, int lane, int w) {
  const int c = lane & 15, g = lane >> 4;
  const float* mod = (const float*)(P.ws + OFF_MOD);
  u32x4 wo[32][2];
  load_w2<32, true>(wo, P.wo, 32 * 512, 512, (w * 1024 + g * 8) * 512 + 32 * n + 2 * c);
  float ga[8];
#pragma unroll
  for (int j = 0; j < 8; ++j) ga[j] = mod[1024 + 32 * n + 8 * g + j];
  for (int s = 0; s < NSEG; ++s) {
    const u32 row = (s * 32 + th * 16 + c) * 512 + 32 * n + 8 * g;
    u32x4 xr = *(const u32x4*)(P.x + row);
    wait_ge<4>(P.ws, OFF_OC + s * 128, 32);
    u32x4 of[32];
#pragma unroll
    for (int k = 0; k < 32; ++k) of[k] = ld4(rw, OFF_O + ((s * 2 + th) * 128 + 32 * w + k) * 1024 + lane * 16);
    f32x4 a0, a1;
#pragma unroll
    for (int k = 0; k < 32; ++k) {
      if (k == 0) { mav0(a0, wo[k][0], of[k]); mav0(a1, wo[k][1], of[k]); } else { mav(a0, wo[k][0], of[k]); mav(a1, wo[k][1], of[k]); }
    }
    fin(a0, a1);
    char* red = lds + (s & 1) * 8192;
    *(f32x4*)(red + (w * 128 + lane) * 16) = a0; *(f32x4*)(red + (w * 128 + 64 + lane) * 16) = a1;
    lds_bar();
    if (w == (s & 3)) {
      a0 = f32x4{0, 0, 0, 0}; a1 = a0;
#pragma unroll
      for (int i = 0; i < 4; ++i) { a0 += *(const f32x4*)(red + (i * 128 + lane) * 16); a1 += *(const f32x4*)(red + (i * 128 + 64 + lane) * 16); }
      u32x4 y;
#pragma unroll
      for (int r = 0; r < 4; ++r) y[r] = pk(bflo(xr[r]) + rbf(ga[2 * r] * rbf(a0[r])), bfhi(xr[r]) + rbf(ga[2 * r + 1] * rbf(a1[r])));
      st4(rw, OFF_X1 + row * 2, y);
      drain();
      if (lane == 0) addw(P.ws, OFF_X1C + (s * 2 + th) * 128);
    }
  }
}

template <int NCH, int LPR> DEV void ln_mod(u32x4 (&v)[NCH], const float* shift, const float* scl, int part) {
  float sum = 0;
#pragma unroll
  for (int i = 0; i < NCH; ++i)
#pragma unroll
    for (int p = 0; p < 4; ++p) sum += bflo(v[i][p]) + bfhi(v[i][p]);
  const float mean = (LPR == 8 ? sum8(sum) : sum16(sum)) * (1.f / 512.f);
  float var = 0;
#pragma unroll
  for (int i = 0; i < NCH; ++i)
#pragma unroll
    for (int p = 0; p < 4; ++p) { float a = bflo(v[i][p]) - mean, b = bfhi(v[i][p]) - mean; var += a * a + b * b; }
  const float rstd = 1.f / sqrtf((LPR == 8 ? sum8(var) : sum16(var)) * (1.f / 512.f) + 1e-5f);
#pragma unroll
  for (int i = 0; i < NCH; ++i)
#pragma unroll
    for (int p = 0; p < 4; ++p) {
      const int d = (i * LPR + part) * 8 + 2 * p;
      f32x2 sh = *(const f32x2*)(shift + d), sc = *(const f32x2*)(scl + d);
      v[i][p] = pk(fmaf((bflo(v[i][p]) - mean) * rstd, rbf(1.f + sc[0]), sh[0]), fmaf((bfhi(v[i][p]) - mean) * rstd, rbf(1.f + sc[1]), sh[1]));
    }
}

DEV void ln1_task(const Ptrs& P, rsrc_t rw, int s, int tid) {
  const float* mod = (const float*)(P.ws + OFF_MOD);
  const int tok = tid >> 3, part = tid & 7;
  u32x4 v[8];
#pragma unroll
  for (int i = 0; i < 8; ++i) v[i] = *(const u32x4*)(P.x + (s * 32 + tok) * 512 + (i * 8 + part) * 8);
  ln_mod<8, 8>(v, mod, mod + 512, part);
#pragma unroll
  for (int i = 0; i < 8; ++i) st4(rw, OFF_H + s * 32768 + (((tok >> 4) * 16 + 2 * i + (part >> 2)) * 64 + (tok & 15) + 16 * (part & 3)) * 16, v[i]);
  drain();
  lds_bar();
  if (tid == 0) stw(P.ws, OFF_HF + s * 128, 1u);
}

DEV void ffn1_role(const Ptrs& P, char* lds, rsrc_t rw, int fg, int th, int tid, int lane, int w) {
  const int c = lane & 15, g = lane >> 4, kf = min(4 * fg + w, NKF - 1);
  const bool act = 4 * fg + w < NKF;
  const float* mod = (const float*)(P.ws + OFF_MOD);
  u32x4 w1[16][2];
  load_w2<16, false>(w1, P.w1, 32 * FF, FF, g * 8 * FF + 32 * kf + 2 * c);
  const u32x4 bb = *(const u32x4*)(P.b1 + 32 * kf + 8 * g);
  const int tok = tid >> 4, part = tid & 15;
  for (int s = 0; s < NSEG; ++s) {
    wait_ge<4>(P.ws, OFF_X1C + (s * 2 + th) * 128, 16);
    u32x4 v[4];
#pragma unroll
    for (int i = 0; i < 4; ++i) v[i] = ld4(rw, OFF_X1 + ((s * 32 + th * 16 + tok) * 512 + (i * 16 + part) * 8) * 2);
    ln_mod<4, 16>(v, mod + 1536, mod + 2048, part);
    char* lh = lds + (s & 1) * 16384;
#pragma unroll
    for (int i = 0; i < 4; ++i) *(u32x4*)(lh + ((4 * i + (part >> 2)) * 64 + (tok ^ (part >> 2)) + 16 * (part & 3)) * 16) = v[i];
    lds_bar();
    f32x4 a0 = {}, a1 = {};
#pragma unroll
    for (int k = 0; k < 16; ++k) {
      u32x4 hf = *(const u32x4*)(lh + (k * 64 + (lane ^ (k & 3))) * 16);
      a0 = mma(w1[k][0], hf, a0); a1 = mma(w1[k][1], hf, a1);
    }
    u32x4 y;
#pragma unroll
    for (int r = 0; r < 4; ++r) {
      float u[2] = {rbf(rbf(a0[r]) + bflo(bb[r])), rbf(rbf(a1[r]) + bfhi(bb[r]))};
#pragma unroll
      for (int e = 0; e < 2; ++e) {
        float z = 1.5957691216057308f * (u[e] + 0.044715f * u[e] * u[e] * u[e]);
        u[e] = u[e] / (1.f + __expf(-z));
      }
      y[r] = pk(u[0], u[1]);
    }
    if (act) st4(rw, OFF_G + ((s * 2 + th) * NKF + kf) * 1024 + lane * 16, y);
    drain();
    lds_bar();
    if (tid == 0) addw(P.ws, OFF_GC + (s * 2 + th) * 128);
  }
}

DEV void ffn2_role(const Ptrs& P, char* lds, rsrc_t rw, int n, int th, int lane, int w) {
  const int c = lane & 15, g = lane >> 4;
  const float* mod = (const float*)(P.ws + OFF_MOD);
  u32x4 w2[11][4];
#pragma unroll
  for (int i = 0; i < 11; ++i) {
    const int kf = min(w + 4 * i, NKF - 1);
    u32x2 t[8];
#pragma unroll
    for (int j = 0; j < 8; ++j) t[j] = *(const u32x2*)(P.w2 + (size_t)(32 * kf + 8 * g + j) * 512 + 64 * n + 4 * c);
#pragma unroll
    for (int p = 0; p < 4; ++p) {
      w2[i][0][p] = ilo(t[2 * p][0], t[2 * p + 1][0]); w2[i][1][p] = ihi(t[2 * p][0], t[2 * p + 1][0]);
      w2[i][2][p] = ilo(t[2 * p][1], t[2 * p + 1][1]); w2[i][3][p] = ihi(t[2 * p][1], t[2 * p + 1][1]);
    }
  }
  const int col = 64 * n + 16 * g + 4 * w;
  const f32x4 gbv = *(const f32x4*)(mod + 2560 + col);
  const u32x2 b2 = *(const u32x2*)(P.b2 + col);
  for (int s = 0; s < NSEG; ++s) {
    wait_ge<4>(P.ws, OFF_GC + (s * 2 + th) * 128, 11);
    u32x4 gf[11];
#pragma unroll
    for (int i = 0; i < 11; ++i) gf[i] = ld4(rw, OFF_G + ((s * 2 + th) * NKF + min(w + 4 * i, NKF - 1)) * 1024 + lane * 16);
    const u32 row = (s * 32 + th * 16 + c) * 512 + col;
    const u32x2 x1 = __builtin_amdgcn_raw_buffer_load_b64(rw, OFF_X1 + row * 2, 0, SC1);
    if (w == 3) gf[10] = u32x4{0, 0, 0, 0};
    f32x4 acc[4] = {};
#pragma unroll
    for (int i = 0; i < 11; ++i)
#pragma unroll
      for (int t = 0; t < 4; ++t) acc[t] = mma(w2[i][t], gf[i], acc[t]);
    char* red = lds + (s & 1) * 16384;
#pragma unroll
    for (int r = 0; r < 4; ++r) *(f32x4*)(red + ((w * 4 + r) * 64 + lane) * 16) = f32x4{acc[0][r], acc[1][r], acc[2][r], acc[3][r]};
    lds_bar();
    f32x4 y = {};
#pragma unroll
    for (int i = 0; i < 4; ++i) y += *(const f32x4*)(red + ((i * 4 + w) * 64 + lane) * 16);
    u32x2 o;
#pragma unroll
    for (int p = 0; p < 2; ++p)
      o[p] = pk(bflo(x1[p]) + rbf(gbv[2 * p] * rbf(rbf(y[2 * p]) + bflo(b2[p]))), bfhi(x1[p]) + rbf(gbv[2 * p + 1] * rbf(rbf(y[2 * p + 1]) + bfhi(b2[p]))));
    *(u32x2*)(P.out + row) = o;
  }
}

__global__ __launch_bounds__(256, 1) void main_kernel(const u16* x, const u16* mem0, const u16* wq, const u16* wk, const u16* wv, const u16* wo,
                                                      const u16* w1, const u16* b1, const u16* w2, const u16* b2, u16* out, char* ws) {
  __shared__ __attribute__((aligned(16))) char lds[LDS_BYTES];
  const Ptrs P = {x, mem0, wq, wk, wv, wo, w1, b1, w2, b2, out, ws};
  const int b = blockIdx.x, tid = threadIdx.x, lane = tid & 63, w = __builtin_amdgcn_readfirstlane(tid >> 6);
  const rsrc_t rw = __builtin_amdgcn_make_buffer_rsrc(ws, 0, WS_BYTES, 0x00020000);
  if (b < 32) {
    chain_role(P, lds, rw, b, lane, w);
  } else if (b < 160) {
    proj_role(P, lds, rw, (b - 32) & 31, (b - 32) >> 5, tid, lane, w);
  } else if (b < 192) {
    oproj_role(P, lds, rw, (b - 160) >> 1, b & 1, lane, w);
  } else {
    ln1_task(P, rw, b - 192, tid);
    if (b < 208) ffn2_role(P, lds, rw, (b - 192) >> 1, b & 1, lane, w);
    else if (b < 230) ffn1_role(P, lds, rw, (b - 208) >> 1, b & 1, tid, lane, w);
  }
}

extern "C" void kernel_launch(void* const* d_in, const int* in_sizes, int n_in, void* d_out, int out_size, void* d_ws, size_t ws_size,
                              hipStream_t stream) {
  const u16* const* in = (const u16* const*)d_in;
  setup_kernel<<<304, 256, 0, stream>>>(in[12], in[13], in[4], in[5], (char*)d_ws);
  main_kernel<<<256, 256, 0, stream>>>(in[0], in[1], in[2], in[3], in[4], in[5], in[8], in[9], in[10], in[11], (u16*)d_out, (char*)d_ws);
}
```

```cpp
#include <hip/hip_runtime.h>
#include <hip/hip_bf16.h>

typedef unsigned u32;
typedef unsigned long long u64;
typedef unsigned short u16;
typedef u32 u32x2 __attribute__((ext_vector_type(2)));
typedef u32 u32x4 __attribute__((ext_vector_type(4)));
typedef u32 u32x8 __attribute__((ext_vector_type(8)));
typedef float f32x4 __attribute__((ext_vector_type(4)));
typedef float f32x2 __attribute__((ext_vector_type(2)));
typedef short s16x4 __attribute__((ext_vector_type(4)));
typedef __bf16 bf16x2 __attribute__((ext_vector_type(2)));
typedef __bf16 bf16x8 __attribute__((ext_vector_type(8)));
typedef __bf16 bf16x16 __attribute__((ext_vector_type(16)));
typedef __amdgpu_buffer_rsrc_t rsrc_t;
#define DEV __device__ __forceinline__

constexpr int NSEG = 64, DM = 512, FF = 1376, NKF = 43;
constexpr u32 OFF_MACC = 0;
constexpr u32 OFF_KVF = 2u << 20;
constexpr u32 OFF_HF = OFF_KVF + 8192;
constexpr u32 OFF_RDY = OFF_HF + 8192;
constexpr u32 OFF_OC = OFF_RDY + 8192;
constexpr u32 OFF_X1C = OFF_OC + 8192;
constexpr u32 OFF_GC = OFF_X1C + 16384;
constexpr u32 ZERO_BYTES = OFF_GC + 16384;
constexpr u32 OFF_MOD = ZERO_BYTES;
constexpr u32 OFF_NRM = OFF_MOD + 12288;
constexpr u32 OFF_H = 3u << 20;
constexpr u32 OFF_KV = 5u << 20;
constexpr u32 OFF_O = 37u << 20;
constexpr u32 OFF_X1 = 53u << 20;
constexpr u32 OFF_G = 55u << 20;
constexpr u32 WS_BYTES = 61u << 20;
constexpr int LDS_BYTES = 163840;
constexpr int SPIN_CAP = 1 << 17;
constexpr int SC1 = 16;
constexpr float SM_C = 0.08838834764831845f * 1.4426950408889634f;

#define STAMP(id)
#define CYC(id)
struct Ptrs {
  const u16 *x, *mem0, *wq, *wk, *wv, *wo, *w1, *b1, *w2, *b2;
  u16* out;
  char* ws;
};

DEV float bflo(u32 v) { return __uint_as_float(v << 16); }
DEV float bfhi(u32 v) { return __uint_as_float(v & 0xffff0000u); }
DEV u32 pk(float a, float b) { bf16x2 v = {(__bf16)a, (__bf16)b}; return __builtin_bit_cast(u32, v); }
DEV float rbf(float a) { return (float)(__bf16)a; }
DEV u32 ilo(u32 a, u32 b) { return __builtin_amdgcn_perm(b, a, 0x05040100u); }
DEV u32 ihi(u32 a, u32 b) { return __builtin_amdgcn_perm(b, a, 0x07060302u); }
DEV f32x4 mma(u32x4 a, u32x4 b, f32x4 c) {
  return __builtin_amdgcn_mfma_f32_16x16x32_bf16(__builtin_bit_cast(bf16x8, a), __builtin_bit_cast(bf16x8, b), c, 0, 0, 0);
}
#define MMA_ASM(NAME, OP, TA, CA, CB) \
  DEV void NAME(f32x4& c, TA a, TA b) { asm("s_nop 1\n\t" OP " %0, %1, %2, %0" : "+v"(c) : CA(a), CB(b)); } \
  DEV void NAME##0(f32x4& c, TA a, TA b) { asm("s_nop 1\n\t" OP " %0, %1, %2, 0" : "=&v"(c) : CA(a), CB(b)); }
MMA_ASM(mvv, "v_mfma_f32_16x16x32_bf16", u32x4, "v", "v")
MMA_ASM(mav, "v_mfma_f32_16x16x32_bf16", u32x4, "a", "v")
MMA_ASM(mva, "v_mfma_f32_16x16x32_bf16", u32x4, "v", "a")
MMA_ASM(m16, "v_mfma_f32_16x16x16_bf16", u32x2, "v", "v")
DEV void fin(f32x4& a) { asm volatile("s_nop 7" : "+v"(a)); }
DEV void fin(f32x4& a, f32x4& b) { asm volatile("s_nop 7" : "+v"(a), "+v"(b)); }
DEV void fin(f32x4& a, f32x4& b, f32x4& c) { asm volatile("s_nop 7" : "+v"(a), "+v"(b), "+v"(c)); }
DEV void fin(f32x4& a, f32x4& b, f32x4& c, f32x4& d) { asm volatile("s_nop 7" : "+v"(a), "+v"(b), "+v"(c), "+v"(d)); }
typedef __attribute__((address_space(3))) char* lptr;
DEV lptr lfix(char* p) { lptr q = (lptr)p; asm volatile("" : "+v"(q)); return q; }
#define LRD(p) (*(const __attribute__((address_space(3))) u32x4*)(p))
#define LWR(p) (*(__attribute__((address_space(3))) u32x4*)(p))
#define LW1(p) (*(__attribute__((address_space(3))) u32*)(p))
DEV f32x4 smma(u32x4 a, u32x8 b, f32x4 c, int idx) {
  return __builtin_amdgcn_smfmac_f32_16x16x64_bf16(__builtin_bit_cast(bf16x8, a), __builtin_bit_cast(bf16x16, b), c, idx, 0, 0);
}
DEV float swapadd(float a, float b) {
  auto t = __builtin_amdgcn_permlane32_swap(__float_as_uint(a), __float_as_uint(b), false, false);
  return __uint_as_float(t[0]) + __uint_as_float(t[1]);
}
DEV float xmax(float x) {
  auto s = __builtin_amdgcn_permlane16_swap(__float_as_uint(x), __float_as_uint(x), false, false);
  x = fmaxf(__uint_as_float(s[0]), __uint_as_float(s[1]));
  auto t = __builtin_amdgcn_permlane32_swap(__float_as_uint(x), __float_as_uint(x), false, false);
  return fmaxf(__uint_as_float(t[0]), __uint_as_float(t[1]));
}
DEV float xsum(float x) {
  auto s = __builtin_amdgcn_permlane16_swap(__float_as_uint(x), __float_as_uint(x), false, false);
  x = __uint_as_float(s[0]) + __uint_as_float(s[1]);
  auto t = __builtin_amdgcn_permlane32_swap(__float_as_uint(x), __float_as_uint(x), false, false);
  return __uint_as_float(t[0]) + __uint_as_float(t[1]);
}
template <int C> DEV float dpp(float x) { return __uint_as_float(__builtin_amdgcn_update_dpp(0, __float_as_uint(x), C, 0xf, 0xf, true)); }
DEV float sum8(float x) { x += dpp<0xB1>(x); x += dpp<0x4E>(x); return x + dpp<0x141>(x); }
DEV float sum16(float x) { x = sum8(x); return x + dpp<0x128>(x); }
DEV float max16(float x) { x = fmaxf(x, dpp<0xB1>(x)); x = fmaxf(x, dpp<0x4E>(x)); x = fmaxf(x, dpp<0x141>(x)); return fmaxf(x, dpp<0x128>(x)); }
DEV void lds_bar() { asm volatile("s_waitcnt lgkmcnt(0)\n\ts_barrier" ::: "memory"); }
#define SB __builtin_amdgcn_sched_barrier(0)
DEV void drain() { asm volatile("s_waitcnt vmcnt(0)" ::: "memory"); }
DEV u32x4 ld4(rsrc_t r, u32 off) { return __builtin_amdgcn_raw_buffer_load_b128(r, off, 0, SC1); }
DEV void st4(rsrc_t r, u32 off, u32x4 v) { __builtin_amdgcn_raw_buffer_store_b128(v, r, off, 0, SC1); }
DEV u32 ldw(const char* ws, u32 off) { return __hip_atomic_load((const u32*)(ws + off), __ATOMIC_RELAXED, __HIP_MEMORY_SCOPE_AGENT); }
DEV void stw(char* ws, u32 off, u32 v) { __hip_atomic_store((u32*)(ws + off), v, __ATOMIC_RELAXED, __HIP_MEMORY_SCOPE_AGENT); }
DEV void addw(char* ws, u32 off) { __hip_atomic_fetch_add((u32*)(ws + off), 1u, __ATOMIC_RELAXED, __HIP_MEMORY_SCOPE_AGENT); }
template <int SLEEP> DEV u32 wait_ge(const char* ws, u32 off, u32 target) {
  u32 v = 0;
  for (int n = 0; n < SPIN_CAP; ++n) {
    v = __builtin_amdgcn_readfirstlane(ldw(ws, off));
    if (v >= target) break;
    __builtin_amdgcn_s_sleep(SLEEP);
  }
  asm volatile("" ::: "memory");
  return v;
}
DEV f32x4 zv() { f32x4 z = {0, 0, 0, 0}; asm volatile("" : "+v"(z)); return z; }
template <class T> DEV void pin_acc(T& v) { asm volatile("" : "+a"(v)); }
template <int NK, bool ACC> DEV void load_w2(u32x4 (&w)[NK][2], const u16* base, u32 kstep, u32 rstride, u32 loff) {
  constexpr int DEPTH = 7;
  u32 t[DEPTH + 1][8];
#pragma unroll
  for (int ks = 0; ks < NK + DEPTH; ++ks) {
    if (ks < NK) {
#pragma unroll
      for (int j = 0; j < 8; ++j) t[ks % (DEPTH + 1)][j] = *(const u32*)(base + (size_t)(ks * kstep + j * rstride) + loff);
    }
    SB;
    if (ks >= DEPTH) {
      const int k = ks - DEPTH;
#pragma unroll
      for (int p = 0; p < 4; ++p) {
        w[k][0][p] = ilo(t[k % (DEPTH + 1)][2 * p], t[k % (DEPTH + 1)][2 * p + 1]); w[k][1][p] = ihi(t[k % (DEPTH + 1)][2 * p], t[k % (DEPTH + 1)][2 * p + 1]);
      }
      if (ACC) { pin_acc(w[k][0]); pin_acc(w[k][1]); }
      SB;
    }
  }
}
DEV u32 dec(u32 lo, u32 hi, float inv) { return pk(fmaf((float)(int)lo, inv, 976.f * inv), (float)(int)hi * inv); }
DEV void poll_macc(rsrc_t r, u32 off, u32x4 (&q)[4]) {
  for (int n = 0; n < SPIN_CAP; ++n) {
    asm volatile("" ::: "memory");
#pragma unroll
    for (int i = 0; i < 4; ++i) q[i] = ld4(r, off + i * 2048);
    u32 m = 32;
#pragma unroll
    for (int i = 0; i < 4; ++i) m &= q[i][0] & q[i][2];
    if (__all(m != 0)) break;
  }
}

__global__ __launch_bounds__(256) void setup_kernel(const u16* c, const u16* wmod, const u16* wv, const u16* wo, char* ws) {
  __shared__ float red[32 * 64];
  __shared__ float sc[512];
  const int b = blockIdx.x, tid = threadIdx.x;
  for (u32 i = b * 256 + tid; i < ZERO_BYTES / 16; i += gridDim.x * 256) ((u32x4*)ws)[i] = u32x4{0, 0, 0, 0};
  float* mod = (float*)(ws + OFF_MOD);
  float* nrm = (float*)(ws + OFF_NRM);
  if (b < 48) {
    u32x4 wr[16];
#pragma unroll
    for (int it = 0; it < 16; ++it) wr[it] = *(const u32x4*)(wmod + (size_t)(it * 32 + (tid >> 3)) * 3072 + b * 64 + (tid & 7) * 8);
    for (int i = tid; i < 512; i += 256) { float v = bflo(c[i]); sc[i] = rbf(v / (1.f + __expf(-v))); }
    __syncthreads();
    float acc[8] = {0, 0, 0, 0, 0, 0, 0, 0};
#pragma unroll
    for (int it = 0; it < 16; ++it) {
      float s = sc[it * 32 + (tid >> 3)];
#pragma unroll
      for (int p = 0; p < 4; ++p) { acc[2 * p] += s * bflo(wr[it][p]); acc[2 * p + 1] += s * bfhi(wr[it][p]); }
    }
#pragma unroll
    for (int j = 0; j < 8; ++j) red[(tid >> 3) * 64 + (tid & 7) * 8 + j] = acc[j];
    __syncthreads();
    if (tid < 64) { float s = 0; for (int i = 0; i < 32; ++i) s += red[i * 64 + tid]; mod[b * 64 + tid] = rbf(s); }
  } else {
    const int h = (b - 48) >> 3, part = (b - 48) & 7;
    u32x4 v[4], o[4];
#pragma unroll
    for (int i = 0; i < 4; ++i) {
      v[i] = *(const u32x4*)(wv + (size_t)(part * 64 + i * 16 + (tid >> 4)) * 4096 + h * 128 + (tid & 15) * 8);
      o[i] = *(const u32x4*)(wo + (size_t)(h * 128 + i * 32 + (tid >> 3)) * 512 + part * 64 + (tid & 7) * 8);
    }
    float a = 0, cs[8] = {0, 0, 0, 0, 0, 0, 0, 0};
#pragma unroll
    for (int i = 0; i < 4; ++i)
#pragma unroll
      for (int p = 0; p < 4; ++p) {
        a += bflo(v[i][p]) * bflo(v[i][p]) + bfhi(v[i][p]) * bfhi(v[i][p]);
        cs[2 * p] += bflo(o[i][p]) * bflo(o[i][p]); cs[2 * p + 1] += bfhi(o[i][p]) * bfhi(o[i][p]);
      }
#pragma unroll
    for (int j = 0; j < 8; ++j) red[(tid >> 3) * 64 + (tid & 7) * 8 + j] = cs[j];
    sc[tid] = a;
    __syncthreads();
    if (tid < 64) {
      float s = 0; for (int i = 0; i < 32; ++i) s += red[i * 64 + tid];
      float t = sc[tid] + sc[tid + 64] + sc[tid + 128] + sc[tid + 192];
      s = xmax(max16(s)); t = xsum(sum16(t));
      if (tid == 0) { nrm[h * 8 + part] = t; nrm[256 + h * 8 + part] = s; }
    }
  }
}

DEV void chain_role(const Ptrs& P, char* lds, rsrc_t rw, int h, int lane, int w) {
  char* L_WV = lds + w * 32768;
  char* L_M = lds + 131072;
  char* L_Q = L_M + 8192;
  char* L_K = L_Q + 2304;
  char* L_O = L_K + 2304;
  const int c = lane & 15, g = lane >> 4;
  const int sidx = (lane & 8) ? 0xEEEE : 0x4444;
  const float* mod = (const float*)(P.ws + OFF_MOD);
  const float* nrm = (const float*)(P.ws + OFF_NRM);
  u32x8 wqk[4][8], wo[8][2];
  {
    const u32 loff = (32 * (g >> 1) + 2 * (g & 1)) * 4096;
    const u16* vb = P.wv + h * 128 + 32 * w;
    const u16* wb = (w < 2 ? P.wq : P.wk) + h * 128 + (w & 1) * 64;
    u32 tv[4][16];
    u32x2 tq[4][16];
#pragma unroll
    for (int s = 0; s < 19; ++s) {
#pragma unroll
      for (int p = 0; p < 16; ++p) {
        const int row = 128 * ((s & 7) >> 1) + 4 * (s & 1) + 64 * (p >> 3) + 16 * ((p >> 1) & 1) + 8 * ((p >> 2) & 1) + (p & 1);
        if (s < 8) tv[s & 3][p] = *(const u32*)(vb + (size_t)row * 4096 + (loff + 2 * c));
        else if (s < 16) tq[s & 3][p] = *(const u32x2*)(wb + (size_t)row * 4096 + (loff + 4 * c));
      }
      SB;
      const int k = s - 3;
      if (k >= 0 && k < 8) {
        u32x8 f0, f1;
#pragma unroll
        for (int p = 0; p < 8; ++p) { f0[p] = ilo(tv[k & 3][2 * p], tv[k & 3][2 * p + 1]); f1[p] = ihi(tv[k & 3][2 * p], tv[k & 3][2 * p + 1]); }
        *(u32x4*)(L_WV + ((k * 2 + 0) * 64 + lane) * 16) = f0.lo; *(u32x4*)(L_WV + ((k * 2 + 1) * 64 + lane) * 16) = f0.hi;
        *(u32x4*)(L_WV + ((16 + k * 2 + 0) * 64 + lane) * 16) = f1.lo; *(u32x4*)(L_WV + ((16 + k * 2 + 1) * 64 + lane) * 16) = f1.hi;
      } else if (k >= 8) {
#pragma unroll
        for (int p = 0; p < 8; ++p) {
          const u32x2 r0 = tq[k & 3][2 * p], r1 = tq[k & 3][2 * p + 1];
          wqk[0][k - 8][p] = ilo(r0[0], r1[0]); wqk[1][k - 8][p] = ihi(r0[0], r1[0]);
          wqk[2][k - 8][p] = ilo(r0[1], r1[1]); wqk[3][k - 8][p] = ihi(r0[1], r1[1]);
        }
#pragma unroll
        for (int t = 0; t < 4; ++t) pin_acc(wqk[t][k - 8]);
      }
      SB;
    }
    const u16* ob = P.wo + (size_t)h * 128 * 512 + 128 * w;
    const u32 ooff = (16 * (g >> 1) + 4 * (g & 1)) * 512 + 8 * c;
    u32x4 to[32];
#pragma unroll
    for (int i = 0; i < 32; ++i) to[i] = *(const u32x4*)(ob + (64 * (i >> 4) + 32 * ((i >> 3) & 1) + 8 * ((i >> 1) & 1) + 2 * ((i >> 2) & 1) + (i & 1)) * 512 + ooff);
    SB;
#pragma unroll
    for (int i = 0; i < 16; ++i)
#pragma unroll
      for (int u = 0; u < 4; ++u) { wo[2 * u][i >> 3][i & 7] = ilo(to[2 * i][u], to[2 * i + 1][u]); wo[2 * u + 1][i >> 3][i & 7] = ihi(to[2 * i][u], to[2 * i + 1][u]); }
  }
  const lptr wol = lfix(lds + 147456 + w * 4096 + lane * 16);
  LWR(wol) = wo[7][0].lo; LWR(wol + 1024) = wo[7][0].hi; LWR(wol + 2048) = wo[7][1].lo; LWR(wol + 3072) = wo[7][1].hi;
  float gb, hx2;
  {
    float a = 0, b = 0;
#pragma unroll
    for (int i = 0; i < 8; ++i) { a += nrm[(lane & 31) * 8 + i]; b = fmaxf(b, nrm[256 + (lane & 31) * 8 + i]); }
    gb = 0.5f * 1.03f * xsum(sum16(sqrtf(a * b)));
    float s2 = 0, cm = 0;
#pragma unroll
    for (int i = 0; i < 8; ++i) { float sa = mod[lane * 8 + i]; s2 += sa * sa; cm = fmaxf(cm, fabsf(1.f + mod[512 + lane * 8 + i])); }
    float hx = 1.01f * (22.6275f * xmax(max16(cm)) + sqrtf(xsum(sum16(s2))));
    hx2 = hx * hx;
  }
  const int mtok = lane >> 3, msub = lane & 7;
  const lptr mw = lfix(L_M + (2 * w * 64 + msub * 8 + (mtok ^ msub)) * 16);
  const lptr mr = lfix(L_M + ((lane >> 3) * 8 + ((lane & 7) ^ (lane >> 3))) * 16);
  {
    const u16* a = P.mem0 + mtok * 512 + 128 * w + 16 * msub;
#pragma unroll
    for (int f = 0; f < 2; ++f) {
      u32x2 x0 = *(const u32x2*)(a + 4 * f), x1 = *(const u32x2*)(a + 4 * f + 8);
      LWR(mw + f * 1024) = u32x4{x0[0], x1[0], x0[1], x1[1]};
    }
  }
  const u32 kvf = OFF_KVF + h * 256;
  u32x4 kx[8], vx[2];
  STAMP(1);
  wait_ge<2>(P.ws, kvf, 1);
  STAMP(2);
#pragma unroll
  for (int i = 0; i < 8; ++i) kx[i] = ld4(rw, OFF_KV + h * 16384 + i * 1024 + lane * 16);
#pragma unroll
  for (int a = 0; a < 2; ++a) vx[a] = ld4(rw, OFF_KV + h * 16384 + 8192 + (2 * w + a) * 1024 + lane * 16);
  u32 fl = ldw(P.ws, kvf + 4);
  const lptr qkw = lfix((w < 2 ? L_Q : L_K) + (8 * (w & 1) + (c >> 1)) * 144 + 64 * (g & 1) + 4 * (c & 1) + 8 * (lane >> 5));
  const lptr qkr = lfix(L_Q + g * 144 + (lane & 7) * 16);
  const lptr wvr = lfix(L_WV + lane * 16);
  lptr mr2 = mr;
  const lptr ow = lfix(L_O + ((4 * w + g) * 8 + (lane & 7)) * 16), orr = lfix(L_O + lane * 16);
  const u32 aoff = (w * 32 + (lane >> 5) * 8 + 4 * (g & 1)) * 256 + c * 8;
  const u32 poff = (w * 32 + mtok) * 256 + msub * 16;
  lds_bar();

  for (int s = 0; s < NSEG - 1; ++s) {
    STAMP(64 + s); CYC(256 + s * 8);
    f32x4 aq[4] = {zv(), zv(), zv(), zv()}, av[2] = {zv(), zv()}, gm, sm, s0, s1;
    u32x4 mf[8];
    u32x8 wb[2][2];
#define LDV(k) if ((k) >= 3) mf[k] = LRD(mr2 + (k) * 1024); _Pragma("unroll") for (int a = 0; a < 2; ++a) { wb[(k) & 1][a].lo = LRD(wvr + (a * 16 + (k) * 2) * 1024); wb[(k) & 1][a].hi = LRD(wvr + (a * 16 + (k) * 2 + 1) * 1024); }
#define MMV(k) { asm volatile("" : "+v"(wb[(k) & 1][0]), "+v"(wb[(k) & 1][1])); av[0] = smma(mf[k], wb[(k) & 1][0], av[0], sidx); av[1] = smma(mf[k], wb[(k) & 1][1], av[1], sidx); }
#pragma unroll
    for (int k = 0; k < 8; ++k) mf[k] = LRD(mr + k * 1024);
    LDV(0);
    SB;
#pragma unroll
    for (int k = 0; k < 8; ++k)
#pragma unroll
      for (int t = 0; t < 4; ++t) aq[t] = smma(mf[k], wqk[t][k], aq[t], sidx);
    SB;
    asm volatile("" : "+v"(mr2));
    MMV(0); SB; LDV(1); SB;
    mvv0(gm, mf[0], mf[0]); mvv(gm, mf[1], mf[1]);
#pragma unroll
    for (int r = 0; r < 4; ++r) LW1(qkw + r * 16) = pk(swapadd(aq[0][r], aq[1][r]), swapadd(aq[2][r], aq[3][r]));
    SB;
    LDV(2); MMV(1); SB;
    lds_bar();
    CYC(256 + s * 8 + 1);
    {
      u32x4 qf[4], kf[4];
      qf[0] = LRD(qkr); qf[1] = LRD(qkr + 576); kf[0] = LRD(qkr + 2304); kf[1] = LRD(qkr + 2304 + 576);
      SB;
      MMV(2); mvv(gm, mf[2], mf[2]); SB;
      qf[2] = LRD(qkr + 1152); qf[3] = LRD(qkr + 1728); kf[2] = LRD(qkr + 2304 + 1152); kf[3] = LRD(qkr + 2304 + 1728);
      SB;
      mvv0(s0, kx[0], qf[0]); mvv0(s1, kx[4], qf[0]); mvv0(sm, kf[0], qf[0]);
      mvv(s0, kx[1], qf[1]); mvv(s1, kx[5], qf[1]); mvv(sm, kf[1], qf[1]);
      SB; LDV(3); SB;
      mvv(s0, kx[2], qf[2]); mvv(s1, kx[6], qf[2]); mvv(sm, kf[2], qf[2]);
      mvv(s0, kx[3], qf[3]); mvv(s1, kx[7], qf[3]); mvv(sm, kf[3], qf[3]);
      SB; LDV(4); SB;
    }
    MMV(3); mvv(gm, mf[3], mf[3]); SB;
    LDV(5); SB;
    asm volatile("" : "+v"(sm), "+v"(s0), "+v"(s1));
    float mx = fmaxf(fmaxf(fmaxf(sm[0], sm[1]), fmaxf(sm[2], sm[3])), fmaxf(fmaxf(fmaxf(s0[0], s0[1]), fmaxf(s0[2], s0[3])), fmaxf(fmaxf(s1[0], s1[1]), fmaxf(s1[2], s1[3]))));
    SB; MMV(4); mvv(gm, mf[4], mf[4]); SB; LDV(6); SB;
    mx = xmax(mx) * SM_C;
    SB; MMV(5); mvv(gm, mf[5], mf[5]); SB; LDV(7); SB;
#pragma unroll
    for (int r = 0; r < 4; ++r) { s0[r] = __builtin_amdgcn_exp2f(fmaf(s0[r], SM_C, -mx)); s1[r] = __builtin_amdgcn_exp2f(fmaf(s1[r], SM_C, -mx)); }
    SB; MMV(6); mvv(gm, mf[6], mf[6]); SB;
    u32x4 px = {pk(s0[0], s0[1]), pk(s0[2], s0[3]), pk(s1[0], s1[1]), pk(s1[2], s1[3])};
#pragma unroll
    for (int r = 0; r < 4; ++r) sm[r] = __builtin_amdgcn_exp2f(fmaf(sm[r], SM_C, -mx));
    SB; MMV(7); SB;
    f32x4 o[2];
    mvv0(o[0], vx[0], px); mvv0(o[1], vx[1], px);
    mvv(gm, mf[7], mf[7]);
    SB;
    u32x2 pm = {pk(sm[0], sm[1]), pk(sm[2], sm[3])};
    float rs = 0.5f * ((sm[0] + sm[1]) + (sm[2] + sm[3])) + ((s0[0] + s0[1]) + (s0[2] + s0[3])) + ((s1[0] + s1[1]) + (s1[2] + s1[3]));
#pragma unroll
    for (int a = 0; a < 2; ++a) m16(o[a], u32x2{pk(av[a][0], av[a][1]), pk(av[a][2], av[a][3])}, pm);
    SB;
    asm volatile("" : "+v"(gm));
    const float n2 = 2.f * xmax(max16(fmaxf(fmaxf(gm[0], gm[1]), fmaxf(gm[2], gm[3]))));
    const float bound = fmaxf(__builtin_amdgcn_sqrtf(fmaxf(n2, hx2)) * gb, 1e-20f), inv = bound * (1.f / 2080374784.f);
    rs = __builtin_amdgcn_rcpf(xsum(rs) * inv);
    fin(o[0], o[1]);
    if (!(lane & 8))
      LWR(ow) = u32x4{pk(o[0][0] * rs, o[1][0] * rs), pk(o[0][1] * rs, o[1][1] * rs), pk(o[0][2] * rs, o[1][2] * rs), pk(o[0][3] * rs, o[1][3] * rs)};
    lds_bar();
    CYC(256 + s * 8 + 2);
    u32x4 of0 = LRD(orr), of1 = LRD(orr + 1024);
    wo[7][0].lo = LRD(wol); wo[7][0].hi = LRD(wol + 1024); wo[7][1].lo = LRD(wol + 2048); wo[7][1].hi = LRD(wol + 3072);
    if (s + 1 < NSEG - 1) {
      if (__builtin_amdgcn_readfirstlane(fl) == 0) wait_ge<2>(P.ws, kvf + 4 * (s + 1), 1);
      const u32 kb = OFF_KV + ((s + 1) * 32 + h) * 16384 + lane * 16;
#pragma unroll
      for (int i = 0; i < 8; ++i) kx[i] = ld4(rw, kb + i * 1024);
#pragma unroll
      for (int a = 0; a < 2; ++a) vx[a] = ld4(rw, kb + 8192 + (2 * w + a) * 1024);
      fl = ldw(P.ws, kvf + 4 * (s + 2));
    }
    SB;
    char* ab = P.ws + OFF_MACC + (s + 1) * 32768;
#pragma unroll
    for (int q = 0; q < 2; ++q) {
      f32x4 t[4];
#pragma unroll
      for (int i = 0; i < 4; ++i) { t[i] = smma(of0, wo[4 * q + i][0], zv(), sidx); t[i] = smma(of1, wo[4 * q + i][1], t[i], sidx); }
#pragma unroll
      for (int r = 0; r < 4; ++r) {
        u32 lo = ((u32)(int)swapadd(t[0][r], t[2][r]) & ~63u) | 1u, hi = (u32)(int)swapadd(t[1][r], t[3][r]);
        __hip_atomic_fetch_add((u64*)(ab + aoff + (q * 16 + r) * 256), ((u64)hi << 32) | lo, __ATOMIC_RELAXED, __HIP_MEMORY_SCOPE_AGENT);
      }
    }
    CYC(256 + s * 8 + 3);
    drain();
    CYC(256 + s * 8 + 4);
    u32x4 q[4];
    poll_macc(rw, OFF_MACC + (s + 1) * 32768 + poff, q);
    CYC(256 + s * 8 + 5);
#pragma unroll
    for (int f = 0; f < 2; ++f)
      LWR(mw + f * 1024) = u32x4{dec(q[2 * f][0], q[2 * f][1], inv), dec(q[2 * f][2], q[2 * f][3], inv), dec(q[2 * f + 1][0], q[2 * f + 1][1], inv), dec(q[2 * f + 1][2], q[2 * f + 1][3], inv)};
    lds_bar();
    if (h == 0 && w == 0 && lane == 0) stw(P.ws, OFF_RDY + (s + 1) * 128, __float_as_uint(inv));
  }
}

DEV void proj_role(const Ptrs& P, char* lds, rsrc_t rw, int h, int r0, int tid, int lane, int w) {
  char* L_H = lds;
  char* L_S = lds + 32768;
  char* L_MT = lds + 81920;
  char* L_KM = lds + 98304;
  const int c = lane & 15, g = lane >> 4;
  u32x4 wq[16][2], wk[16][2], wv[16][2];
  const u32 loff = g * 8 * 4096 + h * 128 + 32 * w + 2 * c;
  load_w2<16, true>(wk, P.wk, 32 * 4096, 4096, loff);
  load_w2<16, true>(wv, P.wv, 32 * 4096, 4096, loff);
  load_w2<16, false>(wq, P.wq, 32 * 4096, 4096, loff);
#pragma unroll
  for (int i = 0; i < 4; ++i) *(u32x4*)(L_MT + (i * 256 + tid) * 16) = u32x4{0, 0, 0, 0};
  for (int i = 0; i <= 16; ++i) {
    if (i < 16) {
      const int s = 4 * i + r0;
      char* slot = L_S + (i & 1) * 24576;
      STAMP(64 + i * 4);
      wait_ge<8>(P.ws, OFF_HF + s * 128, 1);
      u32x4 t[8];
#pragma unroll
      for (int j = 0; j < 8; ++j) t[j] = ld4(rw, OFF_H + s * 32768 + (j * 256 + tid) * 16);
#pragma unroll
      for (int j = 0; j < 8; ++j) *(u32x4*)(L_H + (j * 256 + tid) * 16) = t[j];
      lds_bar();
      const u32 kb = OFF_KV + (s * 32 + h) * 16384 + lane * 16;
      u32x2 vp[2][2];
#pragma unroll
      for (int mt = 0; mt < 2; ++mt) {
        f32x4 ak[2], av[2];
#pragma unroll
        for (int ks = 0; ks < 16; ++ks) {
          u32x4 hf = *(const u32x4*)(L_H + ((mt * 16 + ks) * 64 + lane) * 16);
#pragma unroll
          for (int a = 0; a < 2; ++a) {
            if (ks == 0) { mav0(ak[a], wk[ks][a], hf); mva0(av[a], hf, wv[ks][a]); } else { mav(ak[a], wk[ks][a], hf); mva(av[a], hf, wv[ks][a]); }
          }
        }
        fin(ak[0], ak[1], av[0], av[1]);
        u32x4 kf = {pk(ak[0][0], ak[0][1]), pk(ak[0][2], ak[0][3]), pk(ak[1][0], ak[1][1]), pk(ak[1][2], ak[1][3])};
        st4(rw, kb + (mt * 4 + w) * 1024, kf);
        *(u32x4*)(slot + 8192 + ((mt * 4 + w) * 64 + lane) * 16) = kf;
#pragma unroll
        for (int a = 0; a < 2; ++a) vp[mt][a] = u32x2{pk(av[a][0], av[a][1]), pk(av[a][2], av[a][3])};
      }
#pragma unroll
      for (int a = 0; a < 2; ++a) {
        u32x4 vf = {vp[0][a][0], vp[0][a][1], vp[1][a][0], vp[1][a][1]};
        st4(rw, kb + 8192 + (2 * w + a) * 1024, vf);
        *(u32x4*)(slot + 16384 + ((2 * w + a) * 64 + lane) * 16) = vf;
      }
#pragma unroll
      for (int mt = 0; mt < 2; ++mt) {
        f32x4 aq[2];
#pragma unroll
        for (int ks = 0; ks < 16; ++ks) {
          u32x4 hf = *(const u32x4*)(L_H + ((mt * 16 + ks) * 64 + lane) * 16);
#pragma unroll
          for (int a = 0; a < 2; ++a) { if (ks == 0) mvv0(aq[a], wq[ks][a], hf); else mvv(aq[a], wq[ks][a], hf); }
        }
        fin(aq[0], aq[1]);
        *(u32x4*)(slot + ((mt * 4 + w) * 64 + lane) * 16) = u32x4{pk(aq[0][0], aq[0][1]), pk(aq[0][2], aq[0][3]), pk(aq[1][0], aq[1][1]), pk(aq[1][2], aq[1][3])};
      }
      drain();
      lds_bar();
      if (tid == 0) stw(P.ws, OFF_KVF + h * 256 + s * 4, 1u);
      STAMP(64 + i * 4 + 1);
    }
    if (i >= 1) {
      const int s = 4 * (i - 1) + r0;
      const char* slot = L_S + ((i - 1) & 1) * 24576 + lane * 16;
      f32x4 x00, x01, x11;
#pragma unroll
      for (int k = 0; k < 4; ++k) {
        u32x4 q0 = *(const u32x4*)(slot + k * 1024), q1 = *(const u32x4*)(slot + (4 + k) * 1024);
        u32x4 k0 = *(const u32x4*)(slot + (8 + k) * 1024), k1 = *(const u32x4*)(slot + (12 + k) * 1024);
        if (k == 0) { mvv0(x00, k0, q0); mvv0(x01, k0, q1); mvv0(x11, k1, q1); } else { mvv(x00, k0, q0); mvv(x01, k0, q1); mvv(x11, k1, q1); }
      }
      fin(x00, x01, x11);
      const int tok = lane >> 3, sub = lane & 7;
      if (s == 0) {
#pragma unroll
        for (int j = 0; j < 2; ++j) {
          const int u = j * 256 + tid, t = u >> 6, cg = u & 63;
          *(u32x4*)(L_MT + ((cg >> 2) * 64 + t + 16 * (cg & 3)) * 16) = *(const u32x4*)(P.mem0 + t * 512 + cg * 8);
        }
      } else {
        const float inv = __uint_as_float(wait_ge<4>(P.ws, OFF_RDY + s * 128, 1));
        STAMP(64 + (i - 1) * 4 + 2);
        u32x4 q[4];
        poll_macc(rw, OFF_MACC + s * 32768 + (w * 32 + tok) * 256 + sub * 16, q);
#pragma unroll
        for (int u = 0; u < 4; ++u)
#pragma unroll
          for (int e = 0; e < 2; ++e)
            *(u32*)(L_MT + ((4 * w + (sub >> 1)) * 64 + tok + 16 * ((2 * sub + e) & 3)) * 16 + 4 * u) = dec(q[u][2 * e], q[u][2 * e + 1], inv);
      }
      lds_bar();
      f32x4 ak[2], av[2];
#pragma unroll
      for (int ks = 0; ks < 16; ++ks) {
        u32x4 mf = *(const u32x4*)(L_MT + (ks * 64 + lane) * 16);
#pragma unroll
        for (int a = 0; a < 2; ++a) {
          if (ks == 0) { mav0(ak[a], wk[ks][a], mf); mva0(av[a], mf, wv[ks][a]); } else { mav(ak[a], wk[ks][a], mf); mva(av[a], mf, wv[ks][a]); }
        }
      }
      fin(ak[0], ak[1], av[0], av[1]);
      *(u32x4*)(L_KM + (w * 64 + lane) * 16) = u32x4{pk(ak[0][0], ak[0][1]), pk(ak[0][2], ak[0][3]), pk(ak[1][0], ak[1][1]), pk(ak[1][2], ak[1][3])};
      lds_bar();
      f32x4 m0, m1;
#pragma unroll
      for (int k = 0; k < 4; ++k) {
        u32x4 km = *(const u32x4*)(L_KM + (k * 64 + lane) * 16);
        u32x4 q0 = *(const u32x4*)(slot + k * 1024), q1 = *(const u32x4*)(slot + (4 + k) * 1024);
        if (k == 0) { mvv0(m0, km, q0); mvv0(m1, km, q1); } else { mvv(m0, km, q0); mvv(m1, km, q1); }
      }
      fin(m0, m1);
      const float NEG = -__builtin_inff();
#pragma unroll
      for (int r = 0; r < 4; ++r) {
        if (g >= 2) { m0[r] = NEG; m1[r] = NEG; }
        if (4 * g + r > c) { x00[r] = NEG; x11[r] = NEG; }
      }
      float a0 = xmax(fmaxf(fmaxf(fmaxf(m0[0], m0[1]), fmaxf(m0[2], m0[3])), fmaxf(fmaxf(x00[0], x00[1]), fmaxf(x00[2], x00[3])))) * SM_C;
      float a1 = xmax(fmaxf(fmaxf(fmaxf(fmaxf(m1[0], m1[1]), fmaxf(m1[2], m1[3])), fmaxf(fmaxf(x01[0], x01[1]), fmaxf(x01[2], x01[3]))),
                            fmaxf(fmaxf(x11[0], x11[1]), fmaxf(x11[2], x11[3])))) * SM_C;
      float t0 = 0, t1 = 0;
#pragma unroll
      for (int r = 0; r < 4; ++r) {
        m0[r] = __builtin_amdgcn_exp2f(fmaf(m0[r], SM_C, -a0)); x00[r] = __builtin_amdgcn_exp2f(fmaf(x00[r], SM_C, -a0));
        m1[r] = __builtin_amdgcn_exp2f(fmaf(m1[r], SM_C, -a1)); x01[r] = __builtin_amdgcn_exp2f(fmaf(x01[r], SM_C, -a1)); x11[r] = __builtin_amdgcn_exp2f(fmaf(x11[r], SM_C, -a1));
        t0 += m0[r] + x00[r]; t1 += m1[r] + x01[r] + x11[r];
      }
      t0 = __builtin_amdgcn_rcpf(xsum(t0)); t1 = __builtin_amdgcn_rcpf(xsum(t1));
      u32x2 pm0 = {pk(m0[0] * t0, m0[1] * t0), pk(m0[2] * t0, m0[3] * t0)}, pm1 = {pk(m1[0] * t1, m1[1] * t1), pk(m1[2] * t1, m1[3] * t1)};
      u32x4 px0 = {pk(x00[0] * t0, x00[1] * t0), pk(x00[2] * t0, x00[3] * t0), 0u, 0u};
      u32x4 px1 = {pk(x01[0] * t1, x01[1] * t1), pk(x01[2] * t1, x01[3] * t1), pk(x11[0] * t1, x11[1] * t1), pk(x11[2] * t1, x11[3] * t1)};
      f32x4 o0[2], o1[2];
#pragma unroll
      for (int a = 0; a < 2; ++a) {
        u32x4 vf = *(const u32x4*)(slot + (16 + 2 * w + a) * 1024);
        u32x2 vm = {pk(av[a][0], av[a][1]), pk(av[a][2], av[a][3])};
        mvv0(o0[a], vf, px0); m16(o0[a], vm, pm0);
        mvv0(o1[a], vf, px1); m16(o1[a], vm, pm1);
      }
      fin(o0[0], o0[1], o1[0], o1[1]);
      const u32 ob = OFF_O + (s * 256 + 4 * h + w) * 1024 + lane * 16;
      st4(rw, ob, u32x4{pk(o0[0][0], o0[1][0]), pk(o0[0][1], o0[1][1]), pk(o0[0][2], o0[1][2]), pk(o0[0][3], o0[1][3])});
      st4(rw, ob + 131072, u32x4{pk(o1[0][0], o1[1][0]), pk(o1[0][1], o1[1][1]), pk(o1[0][2], o1[1][2]), pk(o1[0][3], o1[1][3])});
      drain();
      lds_bar();
      if (tid == 0) addw(P.ws, OFF_OC + s * 128);
      STAMP(64 + (i - 1) * 4 + 3);
    }
  }
}

DEV void oproj_role(const Ptrs& P, char* lds, rsrc_t rw, int n, int th, int lane, int w) {
  const int c = lane & 15, g = lane >> 4;
  const float* mod = (const float*)(P.ws + OFF_MOD);
  u32x4 wo[32][2];
  load_w2<32, true>(wo, P.wo, 32 * 512, 512, (w * 1024 + g * 8) * 512 + 32 * n + 2 * c);
  float ga[8];
#pragma unroll
  for (int j = 0; j < 8; ++j) ga[j] = mod[1024 + 32 * n + 8 * g + j];
  for (int s = 0; s < NSEG; ++s) {
    const u32 row = (s * 32 + th * 16 + c) * 512 + 32 * n + 8 * g;
    u32x4 xr = *(const u32x4*)(P.x + row);
    wait_ge<4>(P.ws, OFF_OC + s * 128, 32);
    STAMP(64 + s * 2);
    u32x4 of[32];
#pragma unroll
    for (int k = 0; k < 32; ++k) of[k] = ld4(rw, OFF_O + ((s * 2 + th) * 128 + 32 * w + k) * 1024 + lane * 16);
    SB;
    f32x4 a0, a1;
#pragma unroll
    for (int k = 0; k < 32; ++k) {
      if (k == 0) { mav0(a0, wo[k][0], of[k]); mav0(a1, wo[k][1], of[k]); } else { mav(a0, wo[k][0], of[k]); mav(a1, wo[k][1], of[k]); }
    }
    fin(a0, a1);
    char* red = lds + (s & 1) * 8192;
    *(f32x4*)(red + (w * 128 + lane) * 16) = a0; *(f32x4*)(red + (w * 128 + 64 + lane) * 16) = a1;
    lds_bar();
    STAMP(64 + s * 2 + 1);
    if (w == (s & 3)) {
      a0 = f32x4{0, 0, 0, 0}; a1 = a0;
#pragma unroll
      for (int i = 0; i < 4; ++i) { a0 += *(const f32x4*)(red + (i * 128 + lane) * 16); a1 += *(const f32x4*)(red + (i * 128 + 64 + lane) * 16); }
      u32x4 y;
#pragma unroll
      for (int r = 0; r < 4; ++r) y[r] = pk(bflo(xr[r]) + rbf(ga[2 * r] * rbf(a0[r])), bfhi(xr[r]) + rbf(ga[2 * r + 1] * rbf(a1[r])));
      st4(rw, OFF_X1 + row * 2, y);
      drain();
      if (lane == 0) addw(P.ws, OFF_X1C + (s * 2 + th) * 128);
    }
  }
}

template <int NCH, int LPR> DEV void ln_mod(u32x4 (&v)[NCH], const float* shift, const float* scl, int part) {
  float sum = 0;
#pragma unroll
  for (int i = 0; i < NCH; ++i)
#pragma unroll
    for (int p = 0; p < 4; ++p) sum += bflo(v[i][p]) + bfhi(v[i][p]);
  const float mean = (LPR == 8 ? sum8(sum) : sum16(sum)) * (1.f / 512.f);
  float var = 0;
#pragma unroll
  for (int i = 0; i < NCH; ++i)
#pragma unroll
    for (int p = 0; p < 4; ++p) { float a = bflo(v[i][p]) - mean, b = bfhi(v[i][p]) - mean; var += a * a + b * b; }
  const float rstd = 1.f / sqrtf((LPR == 8 ? sum8(var) : sum16(var)) * (1.f / 512.f) + 1e-5f);
#pragma unroll
  for (int i = 0; i < NCH; ++i)
#pragma unroll
    for (int p = 0; p < 4; ++p) {
      const int d = (i * LPR + part) * 8 + 2 * p;
      f32x2 sh = *(const f32x2*)(shift + d), sc = *(const f32x2*)(scl + d);
      v[i][p] = pk(fmaf((bflo(v[i][p]) - mean) * rstd, rbf(1.f + sc[0]), sh[0]), fmaf((bfhi(v[i][p]) - mean) * rstd, rbf(1.f + sc[1]), sh[1]));
    }
}

DEV void ln1_task(const Ptrs& P, rsrc_t rw, int s, int tid) {
  const float* mod = (const float*)(P.ws + OFF_MOD);
  const int tok = tid >> 3, part = tid & 7;
  u32x4 v[8];
#pragma unroll
  for (int i = 0; i < 8; ++i) v[i] = *(const u32x4*)(P.x + (s * 32 + tok) * 512 + (i * 8 + part) * 8);
  ln_mod<8, 8>(v, mod, mod + 512, part);
#pragma unroll
  for (int i = 0; i < 8; ++i) st4(rw, OFF_H + s * 32768 + (((tok >> 4) * 16 + 2 * i + (part >> 2)) * 64 + (tok & 15) + 16 * (part & 3)) * 16, v[i]);
  drain();
  lds_bar();
  if (tid == 0) stw(P.ws, OFF_HF + s * 128, 1u);
  STAMP(3);
}

DEV void ffn1_role(const Ptrs& P, char* lds, rsrc_t rw, int fg, int th, int tid, int lane, int w) {
  const int c = lane & 15, g = lane >> 4, kf = min(4 * fg + w, NKF - 1);
  const bool act = 4 * fg + w < NKF;
  const float* mod = (const float*)(P.ws + OFF_MOD);
  u32x4 w1[16][2];
  load_w2<16, false>(w1, P.w1, 32 * FF, FF, g * 8 * FF + 32 * kf + 2 * c);
  const u32x4 bb = *(const u32x4*)(P.b1 + 32 * kf + 8 * g);
  const int tok = tid >> 4, part = tid & 15;
  for (int s = 0; s < NSEG; ++s) {
    wait_ge<4>(P.ws, OFF_X1C + (s * 2 + th) * 128, 16);
    STAMP(64 + s * 2);
    u32x4 v[4];
#pragma unroll
    for (int i = 0; i < 4; ++i) v[i] = ld4(rw, OFF_X1 + ((s * 32 + th * 16 + tok) * 512 + (i * 16 + part) * 8) * 2);
    ln_mod<4, 16>(v, mod + 1536, mod + 2048, part);
    char* lh = lds + (s & 1) * 16384;
#pragma unroll
    for (int i = 0; i < 4; ++i) *(u32x4*)(lh + ((4 * i + (part >> 2)) * 64 + (tok ^ (part >> 2)) + 16 * (part & 3)) * 16) = v[i];
    lds_bar();
    f32x4 a0 = {}, a1 = {};
#pragma unroll
    for (int k = 0; k < 16; ++k) {
      u32x4 hf = *(const u32x4*)(lh + (k * 64 + (lane ^ (k & 3))) * 16);
      a0 = mma(w1[k][0], hf, a0); a1 = mma(w1[k][1], hf, a1);
    }
    u32x4 y;
#pragma unroll
    for (int r = 0; r < 4; ++r) {
      float u[2] = {rbf(rbf(a0[r]) + bflo(bb[r])), rbf(rbf(a1[r]) + bfhi(bb[r]))};
#pragma unroll
      for (int e = 0; e < 2; ++e) {
        float z = 1.5957691216057308f * (u[e] + 0.044715f * u[e] * u[e] * u[e]);
        u[e] = u[e] / (1.f + __expf(-z));
      }
      y[r] = pk(u[0], u[1]);
    }
    if (act) st4(rw, OFF_G + ((s * 2 + th) * NKF + kf) * 1024 + lane * 16, y);
    drain();
    lds_bar();
    if (tid == 0) addw(P.ws, OFF_GC + (s * 2 + th) * 128);
    STAMP(64 + s * 2 + 1);
  }
}

DEV void ffn2_role(const Ptrs& P, char* lds, rsrc_t rw, int n, int th, int lane, int w) {
  const int c = lane & 15, g = lane >> 4;
  const float* mod = (const float*)(P.ws + OFF_MOD);
  u32x4 w2[11][4];
#pragma unroll
  for (int i = 0; i < 11; ++i) {
    const int kf = min(w + 4 * i, NKF - 1);
    u32x2 t[8];
#pragma unroll
    for (int j = 0; j < 8; ++j) t[j] = *(const u32x2*)(P.w2 + (size_t)(32 * kf + 8 * g + j) * 512 + 64 * n + 4 * c);
#pragma unroll
    for (int p = 0; p < 4; ++p) {
      w2[i][0][p] = ilo(t[2 * p][0], t[2 * p + 1][0]); w2[i][1][p] = ihi(t[2 * p][0], t[2 * p + 1][0]);
      w2[i][2][p] = ilo(t[2 * p][1], t[2 * p + 1][1]); w2[i][3][p] = ihi(t[2 * p][1], t[2 * p + 1][1]);
    }
  }
  const int col = 64 * n + 16 * g + 4 * w;
  const f32x4 gbv = *(const f32x4*)(mod + 2560 + col);
  const u32x2 b2 = *(const u32x2*)(P.b2 + col);
  for (int s = 0; s < NSEG; ++s) {
    wait_ge<4>(P.ws, OFF_GC + (s * 2 + th) * 128, 11);
    STAMP(64 + s * 2);
    u32x4 gf[11];
#pragma unroll
    for (int i = 0; i < 11; ++i) gf[i] = ld4(rw, OFF_G + ((s * 2 + th) * NKF + min(w + 4 * i, NKF - 1)) * 1024 + lane * 16);
    const u32 row = (s * 32 + th * 16 + c) * 512 + col;
    const u32x2 x1 = __builtin_amdgcn_raw_buffer_load_b64(rw, OFF_X1 + row * 2, 0, SC1);
    SB;
    if (w == 3) gf[10] = u32x4{0, 0, 0, 0};
    f32x4 acc[4] = {};
#pragma unroll
    for (int i = 0; i < 11; ++i)
#pragma unroll
      for (int t = 0; t < 4; ++t) acc[t] = mma(w2[i][t], gf[i], acc[t]);
    char* red = lds + (s & 1) * 16384;
#pragma unroll
    for (int r = 0; r < 4; ++r) *(f32x4*)(red + ((w * 4 + r) * 64 + lane) * 16) = f32x4{acc[0][r], acc[1][r], acc[2][r], acc[3][r]};
    lds_bar();
    f32x4 y = {};
#pragma unroll
    for (int i = 0; i < 4; ++i) y += *(const f32x4*)(red + ((i * 4 + w) * 64 + lane) * 16);
    u32x2 o;
#pragma unroll
    for (int p = 0; p < 2; ++p)
      o[p] = pk(bflo(x1[p]) + rbf(gbv[2 * p] * rbf(rbf(y[2 * p]) + bflo(b2[p]))), bfhi(x1[p]) + rbf(gbv[2 * p + 1] * rbf(rbf(y[2 * p + 1]) + bfhi(b2[p]))));
    *(u32x2*)(P.out + row) = o;
    STAMP(64 + s * 2 + 1);
  }
}

__global__ __launch_bounds__(256, 1) void main_kernel(const u16* x, const u16* mem0, const u16* wq, const u16* wk, const u16* wv, const u16* wo,
                                                      const u16* w1, const u16* b1, const u16* w2, const u16* b2, u16* out, char* ws) {
  __shared__ __attribute__((aligned(16))) char lds[LDS_BYTES];
  const Ptrs P = {x, mem0, wq, wk, wv, wo, w1, b1, w2, b2, out, ws};
  const int b = blockIdx.x, tid = threadIdx.x, lane = tid & 63, w = __builtin_amdgcn_readfirstlane(tid >> 6);
  const rsrc_t rw = __builtin_amdgcn_make_buffer_rsrc(ws, 0, WS_BYTES, 0x00020000);
  STAMP(0);
  if (b < 32) {
    chain_role(P, lds, rw, b, lane, w);
  } else if (b < 160) {
    proj_role(P, lds, rw, (b - 32) & 31, (b - 32) >> 5, tid, lane, w);
  } else if (b < 192) {
    oproj_role(P, lds, rw, (b - 160) >> 1, b & 1, lane, w);
  } else {
    ln1_task(P, rw, b - 192, tid);
    if (b < 208) ffn2_role(P, lds, rw, (b - 192) >> 1, b & 1, lane, w);
    else if (b < 230) ffn1_role(P, lds, rw, (b - 208) >> 1, b & 1, tid, lane, w);
  }
}

extern "C" void kernel_launch(void* const* d_in, const int* in_sizes, int n_in, void* d_out, int out_size, void* d_ws, size_t ws_size,
                              hipStream_t stream) {
  const u16* const* in = (const u16* const*)d_in;
  setup_kernel<<<304, 256, 0, stream>>>(in[12], in[13], in[4], in[5], (char*)d_ws);
  main_kernel<<<256, 256, 0, stream>>>(in[0], in[1], in[2], in[3], in[4], in[5], in[8], in[9], in[10], in[11], (u16*)d_out, (char*)d_ws);
}
```

```cpp
#include <hip/hip_runtime.h>
#include <hip/hip_bf16.h>

typedef unsigned u32;
typedef unsigned long long u64;
typedef unsigned short u16;
typedef u32 u32x2 __attribute__((ext_vector_type(2)));
typedef u32 u32x4 __attribute__((ext_vector_type(4)));
typedef u32 u32x8 __attribute__((ext_vector_type(8)));
typedef float f32x4 __attribute__((ext_vector_type(4)));
typedef float f32x2 __attribute__((ext_vector_type(2)));
typedef short s16x4 __attribute__((ext_vector_type(4)));
typedef __bf16 bf16x2 __attribute__((ext_vector_type(2)));
typedef __bf16 bf16x8 __attribute__((ext_vector_type(8)));
typedef __bf16 bf16x16 __attribute__((ext_vector_type(16)));
typedef __amdgpu_buffer_rsrc_t rsrc_t;
#define DEV __device__ __forceinline__

constexpr int NSEG = 64, DM = 512, FF = 1376, NKF = 43;
constexpr u32 OFF_MACC = 0;
constexpr u32 OFF_KVF = 2u << 20;
constexpr u32 OFF_HF = OFF_KVF + 8192;
constexpr u32 OFF_RDY = OFF_HF + 8192;
constexpr u32 OFF_OC = OFF_RDY + 8192;
constexpr u32 OFF_X1C = OFF_OC + 8192;
constexpr u32 OFF_GC = OFF_X1C + 16384;
constexpr u32 ZERO_BYTES = OFF_GC + 16384;
constexpr u32 OFF_MOD = ZERO_BYTES;
constexpr u32 OFF_NRM = OFF_MOD + 12288;
constexpr u32 OFF_H = 3u << 20;
constexpr u32 OFF_KV = 5u << 20;
constexpr u32 OFF_O = 37u << 20;
constexpr u32 OFF_X1 = 53u << 20;
constexpr u32 OFF_G = 55u << 20;
constexpr u32 WS_BYTES = 61u << 20;
constexpr int LDS_BYTES = 163840;
constexpr int SPIN_CAP = 1 << 17;
constexpr int SC1 = 16;
constexpr float SM_C = 0.08838834764831845f * 1.4426950408889634f;

#define STAMP(id)
#define CYC(id)
struct Ptrs {
  const u16 *x, *mem0, *wq, *wk, *wv, *wo, *w1, *b1, *w2, *b2;
  u16* out;
  char* ws;
};

DEV float bflo(u32 v) { return __uint_as_float(v << 16); }
DEV float bfhi(u32 v) { return __uint_as_float(v & 0xffff0000u); }
DEV u32 pk(float a, float b) { bf16x2 v = {(__bf16)a, (__bf16)b}; return __builtin_bit_cast(u32, v); }
DEV float rbf(float a) { return (float)(__bf16)a; }
DEV u32 ilo(u32 a, u32 b) { return __builtin_amdgcn_perm(b, a, 0x05040100u); }
DEV u32 ihi(u32 a, u32 b) { return __builtin_amdgcn_perm(b, a, 0x07060302u); }
DEV f32x4 mma(u32x4 a, u32x4 b, f32x4 c) {
  return __builtin_amdgcn_mfma_f32_16x16x32_bf16(__builtin_bit_cast(bf16x8, a), __builtin_bit_cast(bf16x8, b), c, 0, 0, 0);
}
#define MMA_ASM(NAME, OP, TA, CA, CB) \
  DEV void NAME(f32x4& c, TA a, TA b) { asm("s_nop 1\n\t" OP " %0, %1, %2, %0" : "+v"(c) : CA(a), CB(b)); } \
  DEV void NAME##0(f32x4& c, TA a, TA b) { asm("s_nop 1\n\t" OP " %0, %1, %2, 0" : "=&v"(c) : CA(a), CB(b)); }
MMA_ASM(mvv, "v_mfma_f32_16x16x32_bf16", u32x4, "v", "v")
MMA_ASM(mav, "v_mfma_f32_16x16x32_bf16", u32x4, "a", "v")
MMA_ASM(mva, "v_mfma_f32_16x16x32_bf16", u32x4, "v", "a")
MMA_ASM(m16, "v_mfma_f32_16x16x16_bf16", u32x2, "v", "v")
DEV void fin(f32x4& a) { asm volatile("s_nop 7" : "+v"(a)); }
DEV void fin(f32x4& a, f32x4& b) { asm volatile("s_nop 7" : "+v"(a), "+v"(b)); }
DEV void fin(f32x4& a, f32x4& b, f32x4& c) { asm volatile("s_nop 7" : "+v"(a), "+v"(b), "+v"(c)); }
DEV void fin(f32x4& a, f32x4& b, f32x4& c, f32x4& d) { asm volatile("s_nop 7" : "+v"(a), "+v"(b), "+v"(c), "+v"(d)); }
typedef __attribute__((address_space(3))) char* lptr;
DEV lptr lfix(char* p) { lptr q = (lptr)p; asm volatile("" : "+v"(q)); return q; }
#define LRD(p) (*(const __attribute__((address_space(3))) u32x4*)(p))
#define LWR(p) (*(__attribute__((address_space(3))) u32x4*)(p))
#define LW1(p) (*(__attribute__((address_space(3))) u32*)(p))
DEV f32x4 smma(u32x4 a, u32x8 b, f32x4 c, int idx) {
  return __builtin_amdgcn_smfmac_f32_16x16x64_bf16(__builtin_bit_cast(bf16x8, a), __builtin_bit_cast(bf16x16, b), c, idx, 0, 0);
}
DEV float swapadd(float a, float b) {
  auto t = __builtin_amdgcn_permlane32_swap(__float_as_uint(a), __float_as_uint(b), false, false);
  return __uint_as_float(t[0]) + __uint_as_float(t[1]);
}
DEV float fmax2(float a, float b) { float r; asm("v_max_f32 %0, %1, %2" : "=v"(r) : "v"(a), "v"(b)); return r; }
DEV float fmax3(float a, float b, float c) { float r; asm("v_max3_f32 %0, %1, %2, %3" : "=v"(r) : "v"(a), "v"(b), "v"(c)); return r; }
DEV float xmax(float x) {
  auto s = __builtin_amdgcn_permlane16_swap(__float_as_uint(x), __float_as_uint(x), false, false);
  x = fmax2(__uint_as_float(s[0]), __uint_as_float(s[1]));
  auto t = __builtin_amdgcn_permlane32_swap(__float_as_uint(x), __float_as_uint(x), false, false);
  return fmax2(__uint_as_float(t[0]), __uint_as_float(t[1]));
}
DEV int ixmax(int x) {
  auto s = __builtin_amdgcn_permlane16_swap((u32)x, (u32)x, false, false);
  x = max((int)s[0], (int)s[1]);
  auto t = __builtin_amdgcn_permlane32_swap((u32)x, (u32)x, false, false);
  return max((int)t[0], (int)t[1]);
}
template <int C> DEV int imaxd(int x) { return max(x, __builtin_amdgcn_update_dpp(x, x, C, 0xf, 0xf, true)); }
DEV int imax16(int x) { return imaxd<0x128>(imaxd<0x141>(imaxd<0x4E>(imaxd<0xB1>(x)))); }
DEV float xsum(float x) {
  auto s = __builtin_amdgcn_permlane16_swap(__float_as_uint(x), __float_as_uint(x), false, false);
  x = __uint_as_float(s[0]) + __uint_as_float(s[1]);
  auto t = __builtin_amdgcn_permlane32_swap(__float_as_uint(x), __float_as_uint(x), false, false);
  return __uint_as_float(t[0]) + __uint_as_float(t[1]);
}
template <int C> DEV float dpp(float x) { return __uint_as_float(__builtin_amdgcn_update_dpp(0, __float_as_uint(x), C, 0xf, 0xf, true)); }
DEV float sum8(float x) { x += dpp<0xB1>(x); x += dpp<0x4E>(x); return x + dpp<0x141>(x); }
DEV float sum16(float x) { x = sum8(x); return x + dpp<0x128>(x); }
DEV float max16(float x) { x = fmaxf(x, dpp<0xB1>(x)); x = fmaxf(x, dpp<0x4E>(x)); x = fmaxf(x, dpp<0x141>(x)); return fmaxf(x, dpp<0x128>(x)); }
DEV void lds_bar() { asm volatile("s_waitcnt lgkmcnt(0)\n\ts_barrier" ::: "memory"); }
#define SB __builtin_amdgcn_sched_barrier(0)
DEV void drain() { asm volatile("s_waitcnt vmcnt(0)" ::: "memory"); }
DEV u32x4 ld4(rsrc_t r, u32 off) { return __builtin_amdgcn_raw_buffer_load_b128(r, off, 0, SC1); }
DEV void st4(rsrc_t r, u32 off, u32x4 v) { __builtin_amdgcn_raw_buffer_store_b128(v, r, off, 0, SC1); }
DEV u32 ldw(const char* ws, u32 off) { return __hip_atomic_load((const u32*)(ws + off), __ATOMIC_RELAXED, __HIP_MEMORY_SCOPE_AGENT); }
DEV void stw(char* ws, u32 off, u32 v) { __hip_atomic_store((u32*)(ws + off), v, __ATOMIC_RELAXED, __HIP_MEMORY_SCOPE_AGENT); }
DEV void addw(char* ws, u32 off) { __hip_atomic_fetch_add((u32*)(ws + off), 1u, __ATOMIC_RELAXED, __HIP_MEMORY_SCOPE_AGENT); }
template <int SLEEP> DEV u32 wait_ge(const char* ws, u32 off, u32 target) {
  u32 v = 0;
  for (int n = 0; n < SPIN_CAP; ++n) {
    v = __builtin_amdgcn_readfirstlane(ldw(ws, off));
    if (v >= target) break;
    __builtin_amdgcn_s_sleep(SLEEP);
  }
  asm volatile("" ::: "memory");
  return v;
}
template <int SLEEP> DEV void wait_wg(const char* ws, u32 off, u32 target, int w) {
  if (w == 0) wait_ge<SLEEP>(ws, off, target);
  lds_bar();
}
DEV f32x4 zv() { f32x4 z = {0, 0, 0, 0}; asm volatile("" : "+v"(z)); return z; }
template <class T> DEV void pin_acc(T& v) { asm volatile("" : "+a"(v)); }
template <int NK, bool ACC> DEV void load_w2(u32x4 (&w)[NK][2], const u16* base, u32 kstep, u32 rstride, u32 loff) {
  constexpr int DEPTH = 7;
  u32 t[DEPTH + 1][8];
#pragma unroll
  for (int ks = 0; ks < NK + DEPTH; ++ks) {
    if (ks < NK) {
#pragma unroll
      for (int j = 0; j < 8; ++j) t[ks % (DEPTH + 1)][j] = *(const u32*)(base + (size_t)(ks * kstep + j * rstride) + loff);
    }
    SB;
    if (ks >= DEPTH) {
      const int k = ks - DEPTH;
#pragma unroll
      for (int p = 0; p < 4; ++p) {
        w[k][0][p] = ilo(t[k % (DEPTH + 1)][2 * p], t[k % (DEPTH + 1)][2 * p + 1]); w[k][1][p] = ihi(t[k % (DEPTH + 1)][2 * p], t[k % (DEPTH + 1)][2 * p + 1]);
      }
      if (ACC) { pin_acc(w[k][0]); pin_acc(w[k][1]); }
      SB;
    }
  }
}
DEV u32 dec(u32 lo, u32 hi, float inv) {
  const f32x2 v = __builtin_elementwise_fma(f32x2{(float)(int)lo, (float)(int)hi}, f32x2{inv, inv}, f32x2{976.f * inv, 0.f});
  return pk(v[0], v[1]);
}
DEV void poll_macc(rsrc_t r, u32 off, u32x4 (&q)[4]) {
  for (int n = 0; n < SPIN_CAP; ++n) {
    asm volatile("" ::: "memory");
#pragma unroll
    for (int i = 0; i < 4; ++i) q[i] = ld4(r, off + i * 2048);
    u32 m = 32;
#pragma unroll
    for (int i = 0; i < 4; ++i) m &= q[i][0] & q[i][2];
    if (__all(m != 0)) break;
  }
}

__global__ __launch_bounds__(256) void setup_kernel(const u16* c, const u16* wmod, const u16* wv, const u16* wo, char* ws) {
  __shared__ float red[32 * 64];
  __shared__ float sc[512];
  const int b = blockIdx.x, tid = threadIdx.x;
  for (u32 i = b * 256 + tid; i < ZERO_BYTES / 16; i += gridDim.x * 256) ((u32x4*)ws)[i] = u32x4{0, 0, 0, 0};
  float* mod = (float*)(ws + OFF_MOD);
  float* nrm = (float*)(ws + OFF_NRM);
  if (b < 48) {
    u32x4 wr[16];
#pragma unroll
    for (int it = 0; it < 16; ++it) wr[it] = *(const u32x4*)(wmod + (size_t)(it * 32 + (tid >> 3)) * 3072 + b * 64 + (tid & 7) * 8);
    for (int i = tid; i < 512; i += 256) { float v = bflo(c[i]); sc[i] = rbf(v / (1.f + __expf(-v))); }
    __syncthreads();
    float acc[8] = {0, 0, 0, 0, 0, 0, 0, 0};
#pragma unroll
    for (int it = 0; it < 16; ++it) {
      float s = sc[it * 32 + (tid >> 3)];
#pragma unroll
      for (int p = 0; p < 4; ++p) { acc[2 * p] += s * bflo(wr[it][p]); acc[2 * p + 1] += s * bfhi(wr[it][p]); }
    }
#pragma unroll
    for (int j = 0; j < 8; ++j) red[(tid >> 3) * 64 + (tid & 7) * 8 + j] = acc[j];
    __syncthreads();
    if (tid < 64) { float s = 0; for (int i = 0; i < 32; ++i) s += red[i * 64 + tid]; mod[b * 64 + tid] = rbf(s); }
  } else {
    const int h = (b - 48) >> 3, part = (b - 48) & 7;
    u32x4 v[4], o[4];
#pragma unroll
    for (int i = 0; i < 4; ++i) {
      v[i] = *(const u32x4*)(wv + (size_t)(part * 64 + i * 16 + (tid >> 4)) * 4096 + h * 128 + (tid & 15) * 8);
      o[i] = *(const u32x4*)(wo + (size_t)(h * 128 + i * 32 + (tid >> 3)) * 512 + part * 64 + (tid & 7) * 8);
    }
    float a = 0, cs[8] = {0, 0, 0, 0, 0, 0, 0, 0};
#pragma unroll
    for (int i = 0; i < 4; ++i)
#pragma unroll
      for (int p = 0; p < 4; ++p) {
        a += bflo(v[i][p]) * bflo(v[i][p]) + bfhi(v[i][p]) * bfhi(v[i][p]);
        cs[2 * p] += bflo(o[i][p]) * bflo(o[i][p]); cs[2 * p + 1] += bfhi(o[i][p]) * bfhi(o[i][p]);
      }
#pragma unroll
    for (int j = 0; j < 8; ++j) red[(tid >> 3) * 64 + (tid & 7) * 8 + j] = cs[j];
    sc[tid] = a;
    __syncthreads();
    if (tid < 64) {
      float s = 0; for (int i = 0; i < 32; ++i) s += red[i * 64 + tid];
      float t = sc[tid] + sc[tid + 64] + sc[tid + 128] + sc[tid + 192];
      s = xmax(max16(s)); t = xsum(sum16(t));
      if (tid == 0) { nrm[h * 8 + part] = t; nrm[256 + h * 8 + part] = s; }
    }
  }
}

DEV void chain_role(const Ptrs& P, char* lds, rsrc_t rw, int h, int lane, int w) {
  char* L_WV = lds + w * 32768;
  char* L_M = lds + 131072;
  char* L_Q = L_M + 8192;
  char* L_K = L_Q + 2304;
  char* L_O = L_K + 2304;
  const int c = lane & 15, g = lane >> 4;
  const int sidx = (lane & 8) ? 0xEEEE : 0x4444;
  const float* mod = (const float*)(P.ws + OFF_MOD);
  const float* nrm = (const float*)(P.ws + OFF_NRM);
  u32x8 wqk[4][8], wo[8][2];
  {
    const u32 loff = (32 * (g >> 1) + 2 * (g & 1)) * 4096;
    const u16* vb = P.wv + h * 128 + 32 * w;
    const u16* wb = (w < 2 ? P.wq : P.wk) + h * 128 + (w & 1) * 64;
    u32 tv[4][16];
    u32x2 tq[4][16];
#pragma unroll
    for (int s = 0; s < 19; ++s) {
#pragma unroll
      for (int p = 0; p < 16; ++p) {
        const int row = 128 * ((s & 7) >> 1) + 4 * (s & 1) + 64 * (p >> 3) + 16 * ((p >> 1) & 1) + 8 * ((p >> 2) & 1) + (p & 1);
        if (s < 8) tv[s & 3][p] = *(const u32*)(vb + (size_t)row * 4096 + (loff + 2 * c));
        else if (s < 16) tq[s & 3][p] = *(const u32x2*)(wb + (size_t)row * 4096 + (loff + 4 * c));
      }
      SB;
      const int k = s - 3;
      if (k >= 0 && k < 8) {
        u32x8 f0, f1;
#pragma unroll
        for (int p = 0; p < 8; ++p) { f0[p] = ilo(tv[k & 3][2 * p], tv[k & 3][2 * p + 1]); f1[p] = ihi(tv[k & 3][2 * p], tv[k & 3][2 * p + 1]); }
        *(u32x4*)(L_WV + ((k * 2 + 0) * 64 + lane) * 16) = f0.lo; *(u32x4*)(L_WV + ((k * 2 + 1) * 64 + lane) * 16) = f0.hi;
        *(u32x4*)(L_WV + ((16 + k * 2 + 0) * 64 + lane) * 16) = f1.lo; *(u32x4*)(L_WV + ((16 + k * 2 + 1) * 64 + lane) * 16) = f1.hi;
      } else if (k >= 8) {
#pragma unroll
        for (int p = 0; p < 8; ++p) {
          const u32x2 r0 = tq[k & 3][2 * p], r1 = tq[k & 3][2 * p + 1];
          wqk[0][k - 8][p] = ilo(r0[0], r1[0]); wqk[1][k - 8][p] = ihi(r0[0], r1[0]);
          wqk[2][k - 8][p] = ilo(r0[1], r1[1]); wqk[3][k - 8][p] = ihi(r0[1], r1[1]);
        }
#pragma unroll
        for (int t = 0; t < 4; ++t) pin_acc(wqk[t][k - 8]);
      }
      SB;
    }
    const u16* ob = P.wo + (size_t)h * 128 * 512 + 128 * w;
    const u32 ooff = (16 * (g >> 1) + 4 * (g & 1)) * 512 + 8 * c;
    u32x4 to[32];
#pragma unroll
    for (int i = 0; i < 32; ++i) to[i] = *(const u32x4*)(ob + (64 * (i >> 4) + 32 * ((i >> 3) & 1) + 8 * ((i >> 1) & 1) + 2 * ((i >> 2) & 1) + (i & 1)) * 512 + ooff);
    SB;
#pragma unroll
    for (int i = 0; i < 16; ++i)
#pragma unroll
      for (int u = 0; u < 4; ++u) { wo[2 * u][i >> 3][i & 7] = ilo(to[2 * i][u], to[2 * i + 1][u]); wo[2 * u + 1][i >> 3][i & 7] = ihi(to[2 * i][u], to[2 * i + 1][u]); }
  }
  const lptr kxr = lfix(lds + 147456 + lane * 16), kxw = lfix(lds + 147456 + w * 2048 + lane * 16);
  LWR(kxw + 8192) = wo[7][1].lo; LWR(kxw + 9216) = wo[7][1].hi;
  float gb, hx2;
  {
    float a = 0, b = 0;
#pragma unroll
    for (int i = 0; i < 8; ++i) { a += nrm[(lane & 31) * 8 + i]; b = fmaxf(b, nrm[256 + (lane & 31) * 8 + i]); }
    gb = 0.5f * 1.03f * xsum(sum16(sqrtf(a * b)));
    float s2 = 0, cm = 0;
#pragma unroll
    for (int i = 0; i < 8; ++i) { float sa = mod[lane * 8 + i]; s2 += sa * sa; cm = fmaxf(cm, fabsf(1.f + mod[512 + lane * 8 + i])); }
    float hx = 1.01f * (22.6275f * xmax(max16(cm)) + sqrtf(xsum(sum16(s2))));
    hx2 = hx * hx;
  }
  const int mtok = lane >> 3, msub = lane & 7;
  const lptr mw = lfix(L_M + (2 * w * 64 + msub * 8 + (mtok ^ msub)) * 16);
  const lptr mr = lfix(L_M + ((lane >> 3) * 8 + ((lane & 7) ^ (lane >> 3))) * 16);
  {
    const u16* a = P.mem0 + mtok * 512 + 128 * w + 16 * msub;
#pragma unroll
    for (int f = 0; f < 2; ++f) {
      u32x2 x0 = *(const u32x2*)(a + 4 * f), x1 = *(const u32x2*)(a + 4 * f + 8);
      LWR(mw + f * 1024) = u32x4{x0[0], x1[0], x0[1], x1[1]};
    }
  }
  const u32 kvf = OFF_KVF + h * 256;
  u32x4 ks[2], vx[2];
  STAMP(1);
  wait_ge<2>(P.ws, kvf, 1);
  STAMP(2);
#pragma unroll
  for (int a = 0; a < 2; ++a) { ks[a] = ld4(rw, OFF_KV + h * 16384 + (2 * w + a) * 1024 + lane * 16); vx[a] = ld4(rw, OFF_KV + h * 16384 + 8192 + (2 * w + a) * 1024 + lane * 16); }
  LWR(kxw) = ks[0]; LWR(kxw + 1024) = ks[1];
  u32 fl = ldw(P.ws, kvf + 4);
  const lptr qkw = lfix((w < 2 ? L_Q : L_K) + (8 * (w & 1) + (c >> 1)) * 144 + 64 * (g & 1) + 4 * (c & 1) + 8 * (lane >> 5));
  const lptr qkr = lfix(L_Q + g * 144 + (lane & 7) * 16);
  const lptr wvr = lfix(L_WV + lane * 16);
  const lptr ow = lfix(L_O + ((4 * w + g) * 8 + (lane & 7)) * 16), orr = lfix(L_O + lane * 16);
  const u32 aoff = (w * 32 + (lane >> 5) * 8 + 4 * (g & 1)) * 256 + c * 8;
  const u32 poff = (w * 32 + mtok) * 256 + msub * 16;
  lds_bar();

  for (int s = 0; s < NSEG - 1; ++s) {
    STAMP(64 + s); CYC(5);
    f32x4 aq[4] = {zv(), zv(), zv(), zv()}, av[2] = {zv(), zv()}, gm, sm, s0, s1;
    u32x4 mf[8];
    u32x8 wb[2][2];
#define LDV(k) _Pragma("unroll") for (int a = 0; a < 2; ++a) { wb[(k) & 1][a].lo = LRD(wvr + (a * 16 + (k) * 2) * 1024); wb[(k) & 1][a].hi = LRD(wvr + (a * 16 + (k) * 2 + 1) * 1024); }
#define MMV(k) { asm volatile("" : "+v"(wb[(k) & 1][0]), "+v"(wb[(k) & 1][1])); av[0] = smma(mf[k], wb[(k) & 1][0], av[0], sidx); av[1] = smma(mf[k], wb[(k) & 1][1], av[1], sidx); }
#define MQK(k) _Pragma("unroll") for (int t = 0; t < 4; ++t) aq[t] = smma(mf[k], wqk[t][k], aq[t], sidx);
#pragma unroll
    for (int k = 0; k < 8; ++k) mf[k] = LRD(mr + k * 1024);
    LDV(0);
    SB;
    MQK(0); SB; LDV(1); SB;
    MQK(1); MQK(2); SB;
    MMV(0); SB; LDV(2); mvv0(gm, mf[0], mf[0]); SB;
    MQK(3); MQK(4); SB;
    MMV(1); SB; LDV(3); mvv(gm, mf[1], mf[1]); SB;
    MQK(5); MQK(6); MQK(7); SB;
    MMV(2); mvv(gm, mf[2], mf[2]); SB;
    LDV(4);
    const u32 e0 = pk(swapadd(aq[0][0], aq[1][0]), swapadd(aq[2][0], aq[3][0])), e1 = pk(swapadd(aq[0][1], aq[1][1]), swapadd(aq[2][1], aq[3][1]));
    LW1(qkw) = e0; LW1(qkw + 16) = e1;
    SB;
    MMV(3); SB;
    const u32 e2 = pk(swapadd(aq[0][2], aq[1][2]), swapadd(aq[2][2], aq[3][2])), e3 = pk(swapadd(aq[0][3], aq[1][3]), swapadd(aq[2][3], aq[3][3]));
    LW1(qkw + 32) = e2; LW1(qkw + 48) = e3;
    SB;
    u32x4 qf[4], kf[4], kx[8];
    LDV(5); kx[0] = LRD(kxr); kx[4] = LRD(kxr + 4096);
    SB;
    mvv(gm, mf[3], mf[3]); mvv(gm, mf[4], mf[4]); mvv(gm, mf[5], mf[5]); mvv(gm, mf[6], mf[6]); mvv(gm, mf[7], mf[7]);
    SB;
    asm volatile("s_waitcnt lgkmcnt(6)\n\ts_barrier" ::: "memory");
    CYC(0);
    qf[0] = LRD(qkr); kf[0] = LRD(qkr + 2304); qf[1] = LRD(qkr + 576); kf[1] = LRD(qkr + 2304 + 576); kx[1] = LRD(kxr + 1024); kx[5] = LRD(kxr + 5120);
    SB;
    MMV(4); SB;
    qf[2] = LRD(qkr + 1152); kf[2] = LRD(qkr + 2304 + 1152); kx[2] = LRD(kxr + 2048); kx[6] = LRD(kxr + 6144);
    SB;
    MMV(5); SB;
    qf[3] = LRD(qkr + 1728); kf[3] = LRD(qkr + 2304 + 1728); kx[3] = LRD(kxr + 3072); kx[7] = LRD(kxr + 7168);
    SB;
    fin(gm);
    int n2 = imax16(max(max(__float_as_int(gm[0]), __float_as_int(gm[1])), max(__float_as_int(gm[2]), __float_as_int(gm[3]))));
    SB;
    mvv0(s0, kx[0], qf[0]); mvv0(s1, kx[4], qf[0]); mvv0(sm, kf[0], qf[0]);
    mvv(s0, kx[1], qf[1]); mvv(s1, kx[5], qf[1]); mvv(sm, kf[1], qf[1]);
    mvv(s0, kx[2], qf[2]); mvv(s1, kx[6], qf[2]); mvv(sm, kf[2], qf[2]);
    SB; LDV(6); SB;
    mvv(s0, kx[3], qf[3]); mvv(s1, kx[7], qf[3]); mvv(sm, kf[3], qf[3]);
    SB; LDV(7); SB;
    n2 = ixmax(n2);
    const float inv = fmaxf(__builtin_amdgcn_sqrtf(fmaxf(2.f * __int_as_float(n2), hx2)) * gb, 1e-20f) * (1.f / 2080374784.f);
    fin(s0, s1, sm);
    float mx = fmax2(fmax3(fmax3(s0[0], s0[1], s0[2]), fmax3(s0[3], s1[0], s1[1]), fmax3(s1[2], s1[3], sm[0])), fmax3(sm[1], sm[2], sm[3]));
    mx = xmax(mx) * SM_C;
    SB; MMV(6); SB;
#pragma unroll
    for (int r = 0; r < 4; ++r) s0[r] = __builtin_amdgcn_exp2f(fmaf(s0[r], SM_C, -mx));
    SB; MMV(7); SB;
#pragma unroll
    for (int r = 0; r < 4; ++r) s1[r] = __builtin_amdgcn_exp2f(fmaf(s1[r], SM_C, -mx));
    u32x4 px = {pk(s0[0], s0[1]), pk(s0[2], s0[3]), pk(s1[0], s1[1]), pk(s1[2], s1[3])};
    f32x4 o[2];
    SB;
    mvv0(o[0], vx[0], px); mvv0(o[1], vx[1], px);
    SB;
#pragma unroll
    for (int r = 0; r < 4; ++r) sm[r] = __builtin_amdgcn_exp2f(fmaf(sm[r], SM_C, -mx));
    u32x2 pm = {pk(sm[0], sm[1]), pk(sm[2], sm[3])};
#pragma unroll
    for (int a = 0; a < 2; ++a) m16(o[a], u32x2{pk(av[a][0], av[a][1]), pk(av[a][2], av[a][3])}, pm);
    SB;
    float rs = 0.5f * ((sm[0] + sm[1]) + (sm[2] + sm[3])) + ((s0[0] + s0[1]) + (s0[2] + s0[3])) + ((s1[0] + s1[1]) + (s1[2] + s1[3]));
    rs = __builtin_amdgcn_rcpf(xsum(rs) * inv);
    asm volatile("s_nop 1" : "+v"(o[0]), "+v"(o[1]), "+v"(rs));
    if (!(lane & 8))
      LWR(ow) = u32x4{pk(o[0][0] * rs, o[1][0] * rs), pk(o[0][1] * rs, o[1][1] * rs), pk(o[0][2] * rs, o[1][2] * rs), pk(o[0][3] * rs, o[1][3] * rs)};
    SB;
    wo[7][1].lo = LRD(kxw + 8192); wo[7][1].hi = LRD(kxw + 9216);
    if (s + 1 < NSEG - 1) {
      if (__builtin_amdgcn_readfirstlane(fl) == 0) wait_ge<2>(P.ws, kvf + 4 * (s + 1), 1);
      const u32 kb = OFF_KV + ((s + 1) * 32 + h) * 16384 + 2 * w * 1024 + lane * 16;
#pragma unroll
      for (int a = 0; a < 2; ++a) { ks[a] = ld4(rw, kb + a * 1024); vx[a] = ld4(rw, kb + 8192 + a * 1024); }
      fl = ldw(P.ws, kvf + 4 * (s + 2));
    }
    f32x4 t[8] = {zv(), zv(), zv(), zv(), zv(), zv(), zv(), zv()};
    lds_bar();
    CYC(1);
    u32x4 of0 = LRD(orr), of1 = LRD(orr + 1024);
    char* ab = P.ws + OFF_MACC + (s + 1) * 32768;
    SB;
#define ENC(q, r) { u32 lo = ((u32)(int)swapadd(t[4 * q][r], t[4 * q + 2][r]) & ~63u) | 1u, hi = (u32)(int)swapadd(t[4 * q + 1][r], t[4 * q + 3][r]); \
      __hip_atomic_fetch_add((u64*)(ab + aoff + (q * 16 + r) * 256), ((u64)hi << 32) | lo, __ATOMIC_RELAXED, __HIP_MEMORY_SCOPE_AGENT); }
#pragma unroll
    for (int i = 0; i < 4; ++i) t[i] = smma(of0, wo[i][0], t[i], sidx);
#pragma unroll
    for (int i = 0; i < 4; ++i) t[i] = smma(of1, wo[i][1], t[i], sidx);
    SB;
#pragma unroll
    for (int r = 0; r < 4; ++r) {
      t[4 + (2 * r & 3)] = smma(r < 2 ? of0 : of1, wo[4 + (2 * r & 3)][r >> 1], t[4 + (2 * r & 3)], sidx);
      t[5 + (2 * r & 3)] = smma(r < 2 ? of0 : of1, wo[5 + (2 * r & 3)][r >> 1], t[5 + (2 * r & 3)], sidx);
      SB;
      ENC(0, r);
      SB;
    }
#pragma unroll
    for (int r = 0; r < 4; ++r) ENC(1, r);
    CYC(2);
    drain();
    LWR(kxw) = ks[0]; LWR(kxw + 1024) = ks[1];
    CYC(3);
    u32x4 q[4];
    poll_macc(rw, OFF_MACC + (s + 1) * 32768 + poff, q);
    CYC(4);
#pragma unroll
    for (int f = 0; f < 2; ++f)
      LWR(mw + f * 1024) = u32x4{dec(q[2 * f][0], q[2 * f][1], inv), dec(q[2 * f][2], q[2 * f][3], inv), dec(q[2 * f + 1][0], q[2 * f + 1][1], inv), dec(q[2 * f + 1][2], q[2 * f + 1][3], inv)};
    lds_bar();
    if (h == 0 && w == 0 && lane == 0) stw(P.ws, OFF_RDY + (s + 1) * 128, __float_as_uint(inv));
  }
}

DEV void proj_role(const Ptrs& P, char* lds, rsrc_t rw, int h, int r0, int tid, int lane, int w) {
  char* L_H = lds;
  char* L_S = lds + 32768;
  char* L_MT = lds + 81920;
  char* L_KM = lds + 98304;
  const int c = lane & 15, g = lane >> 4;
  u32x4 wq[16][2], wk[16][2], wv[16][2];
  const u32 loff = g * 8 * 4096 + h * 128 + 32 * w + 2 * c;
  load_w2<16, true>(wk, P.wk, 32 * 4096, 4096, loff);
  load_w2<16, true>(wv, P.wv, 32 * 4096, 4096, loff);
  load_w2<16, false>(wq, P.wq, 32 * 4096, 4096, loff);
#pragma unroll
  for (int i = 0; i < 4; ++i) *(u32x4*)(L_MT + (i * 256 + tid) * 16) = u32x4{0, 0, 0, 0};
  for (int i = 0; i <= 16; ++i) {
    if (i < 16) {
      const int s = 4 * i + r0;
      char* slot = L_S + (i & 1) * 24576;
      STAMP(64 + i * 4);
      wait_wg<8>(P.ws, OFF_HF + s * 128, 1, w);
      u32x4 t[8];
#pragma unroll
      for (int j = 0; j < 8; ++j) t[j] = ld4(rw, OFF_H + s * 32768 + (j * 256 + tid) * 16);
#pragma unroll
      for (int j = 0; j < 8; ++j) *(u32x4*)(L_H + (j * 256 + tid) * 16) = t[j];
      lds_bar();
      const u32 kb = OFF_KV + (s * 32 + h) * 16384 + lane * 16;
      u32x2 vp[2][2];
#pragma unroll
      for (int mt = 0; mt < 2; ++mt) {
        f32x4 ak[2], av[2];
#pragma unroll
        for (int ks = 0; ks < 16; ++ks) {
          u32x4 hf = *(const u32x4*)(L_H + ((mt * 16 + ks) * 64 + lane) * 16);
#pragma unroll
          for (int a = 0; a < 2; ++a) {
            if (ks == 0) { mav0(ak[a], wk[ks][a], hf); mva0(av[a], hf, wv[ks][a]); } else { mav(ak[a], wk[ks][a], hf); mva(av[a], hf, wv[ks][a]); }
          }
        }
        fin(ak[0], ak[1], av[0], av[1]);
        u32x4 kf = {pk(ak[0][0], ak[0][1]), pk(ak[0][2], ak[0][3]), pk(ak[1][0], ak[1][1]), pk(ak[1][2], ak[1][3])};
        st4(rw, kb + (mt * 4 + w) * 1024, kf);
        *(u32x4*)(slot + 8192 + ((mt * 4 + w) * 64 + lane) * 16) = kf;
#pragma unroll
        for (int a = 0; a < 2; ++a) vp[mt][a] = u32x2{pk(av[a][0], av[a][1]), pk(av[a][2], av[a][3])};
      }
#pragma unroll
      for (int a = 0; a < 2; ++a) {
        u32x4 vf = {vp[0][a][0], vp[0][a][1], vp[1][a][0], vp[1][a][1]};
        st4(rw, kb + 8192 + (2 * w + a) * 1024, vf);
        *(u32x4*)(slot + 16384 + ((2 * w + a) * 64 + lane) * 16) = vf;
      }
#pragma unroll
      for (int mt = 0; mt < 2; ++mt) {
        f32x4 aq[2];
#pragma unroll
        for (int ks = 0; ks < 16; ++ks) {
          u32x4 hf = *(const u32x4*)(L_H + ((mt * 16 + ks) * 64 + lane) * 16);
#pragma unroll
          for (int a = 0; a < 2; ++a) { if (ks == 0) mvv0(aq[a], wq[ks][a], hf); else mvv(aq[a], wq[ks][a], hf); }
        }
        fin(aq[0], aq[1]);
        *(u32x4*)(slot + ((mt * 4 + w) * 64 + lane) * 16) = u32x4{pk(aq[0][0], aq[0][1]), pk(aq[0][2], aq[0][3]), pk(aq[1][0], aq[1][1]), pk(aq[1][2], aq[1][3])};
      }
      drain();
      lds_bar();
      if (tid == 0) stw(P.ws, OFF_KVF + h * 256 + s * 4, 1u);
      STAMP(64 + i * 4 + 1);
    }
    if (i >= 1) {
      const int s = 4 * (i - 1) + r0;
      const char* slot = L_S + ((i - 1) & 1) * 24576 + lane * 16;
      f32x4 x00, x01, x11;
#pragma unroll
      for (int k = 0; k < 4; ++k) {
        u32x4 q0 = *(const u32x4*)(slot + k * 1024), q1 = *(const u32x4*)(slot + (4 + k) * 1024);
        u32x4 k0 = *(const u32x4*)(slot + (8 + k) * 1024), k1 = *(const u32x4*)(slot + (12 + k) * 1024);
        if (k == 0) { mvv0(x00, k0, q0); mvv0(x01, k0, q1); mvv0(x11, k1, q1); } else { mvv(x00, k0, q0); mvv(x01, k0, q1); mvv(x11, k1, q1); }
      }
      fin(x00, x01, x11);
      const int tok = lane >> 3, sub = lane & 7;
      if (s == 0) {
#pragma unroll
        for (int j = 0; j < 2; ++j) {
          const int u = j * 256 + tid, t = u >> 6, cg = u & 63;
          *(u32x4*)(L_MT + ((cg >> 2) * 64 + t + 16 * (cg & 3)) * 16) = *(const u32x4*)(P.mem0 + t * 512 + cg * 8);
        }
      } else {
        wait_wg<8>(P.ws, OFF_RDY + s * 128, 1, w);
        const float inv = __uint_as_float(ldw(P.ws, OFF_RDY + s * 128));
        STAMP(64 + (i - 1) * 4 + 2);
        u32x4 q[4];
        poll_macc(rw, OFF_MACC + s * 32768 + (w * 32 + tok) * 256 + sub * 16, q);
#pragma unroll
        for (int u = 0; u < 4; ++u)
#pragma unroll
          for (int e = 0; e < 2; ++e)
            *(u32*)(L_MT + ((4 * w + (sub >> 1)) * 64 + tok + 16 * ((2 * sub + e) & 3)) * 16 + 4 * u) = dec(q[u][2 * e], q[u][2 * e + 1], inv);
      }
      lds_bar();
      f32x4 ak[2], av[2];
#pragma unroll
      for (int ks = 0; ks < 16; ++ks) {
        u32x4 mf = *(const u32x4*)(L_MT + (ks * 64 + lane) * 16);
#pragma unroll
        for (int a = 0; a < 2; ++a) {
          if (ks == 0) { mav0(ak[a], wk[ks][a], mf); mva0(av[a], mf, wv[ks][a]); } else { mav(ak[a], wk[ks][a], mf); mva(av[a], mf, wv[ks][a]); }
        }
      }
      fin(ak[0], ak[1], av[0], av[1]);
      *(u32x4*)(L_KM + (w * 64 + lane) * 16) = u32x4{pk(ak[0][0], ak[0][1]), pk(ak[0][2], ak[0][3]), pk(ak[1][0], ak[1][1]), pk(ak[1][2], ak[1][3])};
      lds_bar();
      f32x4 m0, m1;
#pragma unroll
      for (int k = 0; k < 4; ++k) {
        u32x4 km = *(const u32x4*)(L_KM + (k * 64 + lane) * 16);
        u32x4 q0 = *(const u32x4*)(slot + k * 1024), q1 = *(const u32x4*)(slot + (4 + k) * 1024);
        if (k == 0) { mvv0(m0, km, q0); mvv0(m1, km, q1); } else { mvv(m0, km, q0); mvv(m1, km, q1); }
      }
      fin(m0, m1);
      const float NEG = -__builtin_inff();
#pragma unroll
      for (int r = 0; r < 4; ++r) {
        if (g >= 2) { m0[r] = NEG; m1[r] = NEG; }
        if (4 * g + r > c) { x00[r] = NEG; x11[r] = NEG; }
      }
      float a0 = xmax(fmaxf(fmaxf(fmaxf(m0[0], m0[1]), fmaxf(m0[2], m0[3])), fmaxf(fmaxf(x00[0], x00[1]), fmaxf(x00[2], x00[3])))) * SM_C;
      float a1 = xmax(fmaxf(fmaxf(fmaxf(fmaxf(m1[0], m1[1]), fmaxf(m1[2], m1[3])), fmaxf(fmaxf(x01[0], x01[1]), fmaxf(x01[2], x01[3]))),
                            fmaxf(fmaxf(x11[0], x11[1]), fmaxf(x11[2], x11[3])))) * SM_C;
      float t0 = 0, t1 = 0;
#pragma unroll
      for (int r = 0; r < 4; ++r) {
        m0[r] = __builtin_amdgcn_exp2f(fmaf(m0[r], SM_C, -a0)); x00[r] = __builtin_amdgcn_exp2f(fmaf(x00[r], SM_C, -a0));
        m1[r] = __builtin_amdgcn_exp2f(fmaf(m1[r], SM_C, -a1)); x01[r] = __builtin_amdgcn_exp2f(fmaf(x01[r], SM_C, -a1)); x11[r] = __builtin_amdgcn_exp2f(fmaf(x11[r], SM_C, -a1));
        t0 += m0[r] + x00[r]; t1 += m1[r] + x01[r] + x11[r];
      }
      t0 = __builtin_amdgcn_rcpf(xsum(t0)); t1 = __builtin_amdgcn_rcpf(xsum(t1));
      u32x2 pm0 = {pk(m0[0] * t0, m0[1] * t0), pk(m0[2] * t0, m0[3] * t0)}, pm1 = {pk(m1[0] * t1, m1[1] * t1), pk(m1[2] * t1, m1[3] * t1)};
      u32x4 px0 = {pk(x00[0] * t0, x00[1] * t0), pk(x00[2] * t0, x00[3] * t0), 0u, 0u};
      u32x4 px1 = {pk(x01[0] * t1, x01[1] * t1), pk(x01[2] * t1, x01[3] * t1), pk(x11[0] * t1, x11[1] * t1), pk(x11[2] * t1, x11[3] * t1)};
      f32x4 o0[2], o1[2];
#pragma unroll
      for (int a = 0; a < 2; ++a) {
        u32x4 vf = *(const u32x4*)(slot + (16 + 2 * w + a) * 1024);
        u32x2 vm = {pk(av[a][0], av[a][1]), pk(av[a][2], av[a][3])};
        mvv0(o0[a], vf, px0); m16(o0[a], vm, pm0);
        mvv0(o1[a], vf, px1); m16(o1[a], vm, pm1);
      }
      fin(o0[0], o0[1], o1[0], o1[1]);
      const u32 ob = OFF_O + (s * 256 + 4 * h + w) * 1024 + lane * 16;
      st4(rw, ob, u32x4{pk(o0[0][0], o0[1][0]), pk(o0[0][1], o0[1][1]), pk(o0[0][2], o0[1][2]), pk(o0[0][3], o0[1][3])});
      st4(rw, ob + 131072, u32x4{pk(o1[0][0], o1[1][0]), pk(o1[0][1], o1[1][1]), pk(o1[0][2], o1[1][2]), pk(o1[0][3], o1[1][3])});
      drain();
      lds_bar();
      if (tid == 0) addw(P.ws, OFF_OC + s * 128);
      STAMP(64 + (i - 1) * 4 + 3);
    }
  }
}

DEV void oproj_role(const Ptrs& P, char* lds, rsrc_t rw, int n, int th, int lane, int w) {
  const int c = lane & 15, g = lane >> 4;
  const float* mod = (const float*)(P.ws + OFF_MOD);
  u32x4 wo[32][2];
  load_w2<32, true>(wo, P.wo, 32 * 512, 512, (w * 1024 + g * 8) * 512 + 32 * n + 2 * c);
  float ga[8];
#pragma unroll
  for (int j = 0; j < 8; ++j) ga[j] = mod[1024 + 32 * n + 8 * g + j];
  for (int s = 0; s < NSEG; ++s) {
    const u32 row = (s * 32 + th * 16 + c) * 512 + 32 * n + 8 * g;
    u32x4 xr = *(const u32x4*)(P.x + row);
    wait_wg<8>(P.ws, OFF_OC + s * 128, 32, w);
    STAMP(64 + s * 2);
    u32x4 of[32];
#pragma unroll
    for (int k = 0; k < 32; ++k) of[k] = ld4(rw, OFF_O + ((s * 2 + th) * 128 + 32 * w + k) * 1024 + lane * 16);
    SB;
    f32x4 a0, a1;
#pragma unroll
    for (int k = 0; k < 32; ++k) {
      if (k == 0) { mav0(a0, wo[k][0], of[k]); mav0(a1, wo[k][1], of[k]); } else { mav(a0, wo[k][0], of[k]); mav(a1, wo[k][1], of[k]); }
    }
    fin(a0, a1);
    char* red = lds + (s & 1) * 8192;
    *(f32x4*)(red + (w * 128 + lane) * 16) = a0; *(f32x4*)(red + (w * 128 + 64 + lane) * 16) = a1;
    lds_bar();
    STAMP(64 + s * 2 + 1);
    if (w == (s & 3)) {
      a0 = f32x4{0, 0, 0, 0}; a1 = a0;
#pragma unroll
      for (int i = 0; i < 4; ++i) { a0 += *(const f32x4*)(red + (i * 128 + lane) * 16); a1 += *(const f32x4*)(red + (i * 128 + 64 + lane) * 16); }
      u32x4 y;
#pragma unroll
      for (int r = 0; r < 4; ++r) y[r] = pk(bflo(xr[r]) + rbf(ga[2 * r] * rbf(a0[r])), bfhi(xr[r]) + rbf(ga[2 * r + 1] * rbf(a1[r])));
      st4(rw, OFF_X1 + row * 2, y);
      drain();
      if (lane == 0) addw(P.ws, OFF_X1C + (s * 2 + th) * 128);
    }
  }
}

template <int NCH, int LPR> DEV void ln_coef(u32x4 (&sh)[NCH], u32x4 (&sc)[NCH], const float* shift, const float* scl, int part) {
#pragma unroll
  for (int i = 0; i < NCH; ++i)
#pragma unroll
    for (int p = 0; p < 4; ++p) {
      const int d = (i * LPR + part) * 8 + 2 * p;
      const f32x2 a = *(const f32x2*)(shift + d), b = *(const f32x2*)(scl + d);
      sh[i][p] = pk(a[0], a[1]); sc[i][p] = pk(1.f + b[0], 1.f + b[1]);
    }
}
template <int NCH, int LPR> DEV void ln_mod(u32x4 (&v)[NCH], const u32x4 (&sh)[NCH], const u32x4 (&sc)[NCH]) {
  float sum = 0;
#pragma unroll
  for (int i = 0; i < NCH; ++i)
#pragma unroll
    for (int p = 0; p < 4; ++p) sum += bflo(v[i][p]) + bfhi(v[i][p]);
  const float mean = (LPR == 8 ? sum8(sum) : sum16(sum)) * (1.f / 512.f);
  float var = 0;
#pragma unroll
  for (int i = 0; i < NCH; ++i)
#pragma unroll
    for (int p = 0; p < 4; ++p) { float a = bflo(v[i][p]) - mean, b = bfhi(v[i][p]) - mean; var += a * a + b * b; }
  const float rstd = 1.f / sqrtf((LPR == 8 ? sum8(var) : sum16(var)) * (1.f / 512.f) + 1e-5f);
#pragma unroll
  for (int i = 0; i < NCH; ++i)
#pragma unroll
    for (int p = 0; p < 4; ++p)
      v[i][p] = pk(fmaf((bflo(v[i][p]) - mean) * rstd, bflo(sc[i][p]), bflo(sh[i][p])), fmaf((bfhi(v[i][p]) - mean) * rstd, bfhi(sc[i][p]), bfhi(sh[i][p])));
}

DEV void ln1_task(const Ptrs& P, rsrc_t rw, int s, int tid) {
  const float* mod = (const float*)(P.ws + OFF_MOD);
  const int tok = tid >> 3, part = tid & 7;
  u32x4 v[8];
#pragma unroll
  for (int i = 0; i < 8; ++i) v[i] = *(const u32x4*)(P.x + (s * 32 + tok) * 512 + (i * 8 + part) * 8);
  u32x4 sh[8], sc[8];
  ln_coef<8, 8>(sh, sc, mod, mod + 512, part);
  ln_mod<8, 8>(v, sh, sc);
#pragma unroll
  for (int i = 0; i < 8; ++i) st4(rw, OFF_H + s * 32768 + (((tok >> 4) * 16 + 2 * i + (part >> 2)) * 64 + (tok & 15) + 16 * (part & 3)) * 16, v[i]);
  drain();
  lds_bar();
  if (tid == 0) stw(P.ws, OFF_HF + s * 128, 1u);
  STAMP(3);
}

DEV void ffn1_role(const Ptrs& P, char* lds, rsrc_t rw, int fg, int th, int par, int tid, int lane, int w) {
  const int c = lane & 15, g = lane >> 4, kf = min(4 * fg + w, NKF - 1);
  const bool act = 4 * fg + w < NKF;
  const float* mod = (const float*)(P.ws + OFF_MOD);
  u32x4 w1[16][2];
  load_w2<16, false>(w1, P.w1, 32 * FF, FF, g * 8 * FF + 32 * kf + 2 * c);
  const u32x4 bb = *(const u32x4*)(P.b1 + 32 * kf + 8 * g);
  const int tok = tid >> 4, part = tid & 15;
  u32x4 sh[4], sc[4];
  ln_coef<4, 16>(sh, sc, mod + 1536, mod + 2048, part);
  for (int s = par; s < NSEG; s += 2) {
    wait_wg<8>(P.ws, OFF_X1C + (s * 2 + th) * 128, 16, w);
    STAMP(64 + s * 2);
    u32x4 v[4];
#pragma unroll
    for (int i = 0; i < 4; ++i) v[i] = ld4(rw, OFF_X1 + ((s * 32 + th * 16 + tok) * 512 + (i * 16 + part) * 8) * 2);
    ln_mod<4, 16>(v, sh, sc);
    char* lh = lds;
#pragma unroll
    for (int i = 0; i < 4; ++i) *(u32x4*)(lh + ((4 * i + (part >> 2)) * 64 + (tok ^ (part >> 2)) + 16 * (part & 3)) * 16) = v[i];
    lds_bar();
    f32x4 a0 = {}, a1 = {};
    u32x4 hf[16];
#pragma unroll
    for (int k = 0; k < 16; ++k) hf[k] = *(const u32x4*)(lh + (k * 64 + (lane ^ (k & 3))) * 16);
    SB;
#pragma unroll
    for (int k = 0; k < 16; ++k) { a0 = mma(w1[k][0], hf[k], a0); a1 = mma(w1[k][1], hf[k], a1); }
    u32x4 y;
#pragma unroll
    for (int r = 0; r < 4; ++r) {
      float u[2] = {rbf(rbf(a0[r]) + bflo(bb[r])), rbf(rbf(a1[r]) + bfhi(bb[r]))};
#pragma unroll
      for (int e = 0; e < 2; ++e) {
        float z = 1.5957691216057308f * (u[e] + 0.044715f * u[e] * u[e] * u[e]);
        u[e] = u[e] * __builtin_amdgcn_rcpf(1.f + __expf(-z));
      }
      y[r] = pk(u[0], u[1]);
    }
    if (act) st4(rw, OFF_G + ((s * 2 + th) * NKF + kf) * 1024 + lane * 16, y);
    drain();
    lds_bar();
    if (tid == 0) addw(P.ws, OFF_GC + (s * 2 + th) * 128);
    STAMP(64 + s * 2 + 1);
  }
}

DEV void ffn2_role(const Ptrs& P, char* lds, rsrc_t rw, int n, int th, int lane, int w) {
  const int c = lane & 15, g = lane >> 4;
  const float* mod = (const float*)(P.ws + OFF_MOD);
  u32x4 w2[11][4];
#pragma unroll
  for (int i = 0; i < 11; ++i) {
    const int kf = min(w + 4 * i, NKF - 1);
    u32x2 t[8];
#pragma unroll
    for (int j = 0; j < 8; ++j) t[j] = *(const u32x2*)(P.w2 + (size_t)(32 * kf + 8 * g + j) * 512 + 64 * n + 4 * c);
#pragma unroll
    for (int p = 0; p < 4; ++p) {
      w2[i][0][p] = ilo(t[2 * p][0], t[2 * p + 1][0]); w2[i][1][p] = ihi(t[2 * p][0], t[2 * p + 1][0]);
      w2[i][2][p] = ilo(t[2 * p][1], t[2 * p + 1][1]); w2[i][3][p] = ihi(t[2 * p][1], t[2 * p + 1][1]);
    }
  }
  const int col = 64 * n + 16 * g + 4 * w;
  const f32x4 gbv = *(const f32x4*)(mod + 2560 + col);
  const u32x2 b2 = *(const u32x2*)(P.b2 + col);
  for (int s = 0; s < NSEG; ++s) {
    wait_wg<8>(P.ws, OFF_GC + (s * 2 + th) * 128, 11, w);
    STAMP(64 + s * 2);
    u32x4 gf[11];
#pragma unroll
    for (int i = 0; i < 11; ++i) gf[i] = ld4(rw, OFF_G + ((s * 2 + th) * NKF + min(w + 4 * i, NKF - 1)) * 1024 + lane * 16);
    const u32 row = (s * 32 + th * 16 + c) * 512 + col;
    const u32x2 x1 = __builtin_amdgcn_raw_buffer_load_b64(rw, OFF_X1 + row * 2, 0, SC1);
    SB;
    if (w == 3) gf[10] = u32x4{0, 0, 0, 0};
    f32x4 acc[4] = {};
#pragma unroll
    for (int i = 0; i < 11; ++i)
#pragma unroll
      for (int t = 0; t < 4; ++t) acc[t] = mma(w2[i][t], gf[i], acc[t]);
    char* red = lds + (s & 1) * 16384;
#pragma unroll
    for (int r = 0; r < 4; ++r) *(f32x4*)(red + ((w * 4 + r) * 64 + lane) * 16) = f32x4{acc[0][r], acc[1][r], acc[2][r], acc[3][r]};
    lds_bar();
    f32x4 y = {};
#pragma unroll
    for (int i = 0; i < 4; ++i) y += *(const f32x4*)(red + ((i * 4 + w) * 64 + lane) * 16);
    u32x2 o;
#pragma unroll
    for (int p = 0; p < 2; ++p)
      o[p] = pk(bflo(x1[p]) + rbf(gbv[2 * p] * rbf(rbf(y[2 * p]) + bflo(b2[p]))), bfhi(x1[p]) + rbf(gbv[2 * p + 1] * rbf(rbf(y[2 * p + 1]) + bfhi(b2[p]))));
    *(u32x2*)(P.out + row) = o;
    STAMP(64 + s * 2 + 1);
  }
}

__global__ __launch_bounds__(256, 1) void main_kernel(const u16* x, const u16* mem0, const u16* wq, const u16* wk, const u16* wv, const u16* wo,
                                                      const u16* w1, const u16* b1, const u16* w2, const u16* b2, u16* out, char* ws) {
  __shared__ __attribute__((aligned(16))) char lds[LDS_BYTES];
  const Ptrs P = {x, mem0, wq, wk, wv, wo, w1, b1, w2, b2, out, ws};
  const int b = blockIdx.x, tid = threadIdx.x, lane = tid & 63, w = __builtin_amdgcn_readfirstlane(tid >> 6);
  const rsrc_t rw = __builtin_amdgcn_make_buffer_rsrc(ws, 0, WS_BYTES, 0x00020000);
  STAMP(0);
  if (b < 32) {
    chain_role(P, lds, rw, b, lane, w);
  } else if (b < 160) {
    proj_role(P, lds, rw, (b - 32) & 31, (b - 32) >> 5, tid, lane, w);
  } else if (b < 192) {
    oproj_role(P, lds, rw, (b - 160) >> 1, b & 1, lane, w);
  } else {
    ln1_task(P, rw, b - 192, tid);
    if (b < 208) ffn2_role(P, lds, rw, (b - 192) >> 1, b & 1, lane, w);
    else if (b < 252) ffn1_role(P, lds, rw, ((b - 208) % 22) >> 1, b & 1, (b - 208) / 22, tid, lane, w);
  }
}

extern "C" void kernel_launch(void* const* d_in, const int* in_sizes, int n_in, void* d_out, int out_size, void* d_ws, size_t ws_size,
                              hipStream_t stream) {
  const u16* const* in = (const u16* const*)d_in;
  setup_kernel<<<304, 256, 0, stream>>>(in[12], in[13], in[4], in[5], (char*)d_ws);
  main_kernel<<<256, 256, 0, stream>>>(in[0], in[1], in[2], in[3], in[4], in[5], in[8], in[9], in[10], in[11], (u16*)d_out, (char*)d_ws);
}
```

```cpp
#include <hip/hip_runtime.h>
#include <hip/hip_bf16.h>

typedef unsigned u32;
typedef unsigned long long u64;
typedef unsigned short u16;
typedef u32 u32x2 __attribute__((ext_vector_type(2)));
typedef u32 u32x4 __attribute__((ext_vector_type(4)));
typedef u32 u32x8 __attribute__((ext_vector_type(8)));
typedef float f32x4 __attribute__((ext_vector_type(4)));
typedef float f32x2 __attribute__((ext_vector_type(2)));
typedef short s16x4 __attribute__((ext_vector_type(4)));
typedef __bf16 bf16x2 __attribute__((ext_vector_type(2)));
typedef __bf16 bf16x8 __attribute__((ext_vector_type(8)));
typedef __bf16 bf16x16 __attribute__((ext_vector_type(16)));
typedef __amdgpu_buffer_rsrc_t rsrc_t;
#define DEV __device__ __forceinline__

constexpr int NSEG = 64, DM = 512, FF = 1376, NKF = 43;
constexpr u32 OFF_MACC = 0;
constexpr u32 OFF_KVF = 2u << 20;
constexpr u32 OFF_HF = OFF_KVF + 8192;
constexpr u32 OFF_RDY = OFF_HF + 8192;
constexpr u32 OFF_OC = OFF_RDY + 8192;
constexpr u32 OFF_X1C = OFF_OC + 8192;
constexpr u32 OFF_GC = OFF_X1C + 16384;
constexpr u32 ZERO_BYTES = OFF_GC + 16384;
constexpr u32 OFF_MOD = ZERO_BYTES;
constexpr u32 OFF_NRM = OFF_MOD + 12288;
constexpr u32 OFF_H = 3u << 20;
constexpr u32 OFF_KV = 5u << 20;
constexpr u32 OFF_O = 37u << 20;
constexpr u32 OFF_X1 = 53u << 20;
constexpr u32 OFF_G = 55u << 20;
constexpr u32 WS_BYTES = 61u << 20;
constexpr int LDS_BYTES = 163840;
constexpr int SPIN_CAP = 1 << 17;
constexpr int SC1 = 16;
constexpr float SM_C = 0.08838834764831845f * 1.4426950408889634f;

#define STAMP(id)
#define CYC(id)
struct Ptrs {
  const u16 *x, *mem0, *wq, *wk, *wv, *wo, *w1, *b1, *w2, *b2;
  u16* out;
  char* ws;
};

DEV float bflo(u32 v) { return __uint_as_float(v << 16); }
DEV float bfhi(u32 v) { return __uint_as_float(v & 0xffff0000u); }
DEV u32 pk(float a, float b) { bf16x2 v = {(__bf16)a, (__bf16)b}; return __builtin_bit_cast(u32, v); }
DEV float rbf(float a) { return (float)(__bf16)a; }
DEV u32 ilo(u32 a, u32 b) { return __builtin_amdgcn_perm(b, a, 0x05040100u); }
DEV u32 ihi(u32 a, u32 b) { return __builtin_amdgcn_perm(b, a, 0x07060302u); }
DEV f32x4 mma(u32x4 a, u32x4 b, f32x4 c) {
  return __builtin_amdgcn_mfma_f32_16x16x32_bf16(__builtin_bit_cast(bf16x8, a), __builtin_bit_cast(bf16x8, b), c, 0, 0, 0);
}
#define MMA_ASM(NAME, OP, TA, CA, CB) \
  DEV void NAME(f32x4& c, TA a, TA b) { asm("s_nop 1\n\t" OP " %0, %1, %2, %0" : "+v"(c) : CA(a), CB(b)); } \
  DEV void NAME##0(f32x4& c, TA a, TA b) { asm("s_nop 1\n\t" OP " %0, %1, %2, 0" : "=&v"(c) : CA(a), CB(b)); }
DEV void mld(f32x4& c, u32x4 a, u32x4 b) { asm("v_mfma_f32_16x16x32_bf16 %0, %1, %2, %0" : "+v"(c) : "v"(a), "v"(b)); }
DEV void mld0(f32x4& c, u32x4 a, u32x4 b) { asm("v_mfma_f32_16x16x32_bf16 %0, %1, %2, 0" : "=&v"(c) : "v"(a), "v"(b)); }
MMA_ASM(mvv, "v_mfma_f32_16x16x32_bf16", u32x4, "v", "v")
MMA_ASM(mav, "v_mfma_f32_16x16x32_bf16", u32x4, "a", "v")
MMA_ASM(mva, "v_mfma_f32_16x16x32_bf16", u32x4, "v", "a")
MMA_ASM(m16, "v_mfma_f32_16x16x16_bf16", u32x2, "v", "v")
DEV void fin(f32x4& a) { asm volatile("s_nop 7" : "+v"(a)); }
DEV void fin(f32x4& a, f32x4& b) { asm volatile("s_nop 7" : "+v"(a), "+v"(b)); }
DEV void fin(f32x4& a, f32x4& b, f32x4& c) { asm volatile("s_nop 7" : "+v"(a), "+v"(b), "+v"(c)); }
DEV void fin(f32x4& a, f32x4& b, f32x4& c, f32x4& d) { asm volatile("s_nop 7" : "+v"(a), "+v"(b), "+v"(c), "+v"(d)); }
typedef __attribute__((address_space(3))) char* lptr;
DEV lptr lfix(char* p) { lptr q = (lptr)p; asm volatile("" : "+v"(q)); return q; }
#define LRD(p) (*(const __attribute__((address_space(3))) u32x4*)(p))
#define LWR(p) (*(__attribute__((address_space(3))) u32x4*)(p))
#define LW1(p) (*(__attribute__((address_space(3))) u32*)(p))
DEV f32x4 smma(u32x4 a, u32x8 b, f32x4 c, int idx) {
  return __builtin_amdgcn_smfmac_f32_16x16x64_bf16(__builtin_bit_cast(bf16x8, a), __builtin_bit_cast(bf16x16, b), c, idx, 0, 0);
}
DEV void smld(f32x4& c, u32x4 a, u32x8 b, int idx) {
  asm volatile("v_smfmac_f32_16x16x64_bf16 %0, %1, %2, %3" : "+v"(c) : "v"(a), "v"(b), "v"(idx));
}
DEV float swapadd(float a, float b) {
  auto t = __builtin_amdgcn_permlane32_swap(__float_as_uint(a), __float_as_uint(b), false, false);
  float r;
  asm("v_add_f32 %0, %1, %2" : "=v"(r) : "v"(t[0]), "v"(t[1]));
  return r;
}
DEV float fmax2(float a, float b) { float r; asm("v_max_f32 %0, %1, %2" : "=v"(r) : "v"(a), "v"(b)); return r; }
DEV float fmax3(float a, float b, float c) { float r; asm("v_max3_f32 %0, %1, %2, %3" : "=v"(r) : "v"(a), "v"(b), "v"(c)); return r; }
DEV float xmax(float x) {
  auto s = __builtin_amdgcn_permlane16_swap(__float_as_uint(x), __float_as_uint(x), false, false);
  x = fmax2(__uint_as_float(s[0]), __uint_as_float(s[1]));
  auto t = __builtin_amdgcn_permlane32_swap(__float_as_uint(x), __float_as_uint(x), false, false);
  return fmax2(__uint_as_float(t[0]), __uint_as_float(t[1]));
}
DEV int ixmax(int x) {
  auto s = __builtin_amdgcn_permlane16_swap((u32)x, (u32)x, false, false);
  x = max((int)s[0], (int)s[1]);
  auto t = __builtin_amdgcn_permlane32_swap((u32)x, (u32)x, false, false);
  return max((int)t[0], (int)t[1]);
}
template <int C> DEV int imaxd(int x) { return max(x, __builtin_amdgcn_update_dpp(x, x, C, 0xf, 0xf, true)); }
DEV int imax16(int x) { return imaxd<0x128>(imaxd<0x141>(imaxd<0x4E>(imaxd<0xB1>(x)))); }
DEV float xsum(float x) {
  auto s = __builtin_amdgcn_permlane16_swap(__float_as_uint(x), __float_as_uint(x), false, false);
  x = __uint_as_float(s[0]) + __uint_as_float(s[1]);
  auto t = __builtin_amdgcn_permlane32_swap(__float_as_uint(x), __float_as_uint(x), false, false);
  return __uint_as_float(t[0]) + __uint_as_float(t[1]);
}
template <int C> DEV float dpp(float x) { return __uint_as_float(__builtin_amdgcn_update_dpp(0, __float_as_uint(x), C, 0xf, 0xf, true)); }
DEV float sum8(float x) { x += dpp<0xB1>(x); x += dpp<0x4E>(x); return x + dpp<0x141>(x); }
DEV float sum16(float x) { x = sum8(x); return x + dpp<0x128>(x); }
DEV float max16(float x) { x = fmaxf(x, dpp<0xB1>(x)); x = fmaxf(x, dpp<0x4E>(x)); x = fmaxf(x, dpp<0x141>(x)); return fmaxf(x, dpp<0x128>(x)); }
DEV void lds_bar() { asm volatile("s_waitcnt lgkmcnt(0)\n\ts_barrier" ::: "memory"); }
#define SB __builtin_amdgcn_sched_barrier(0)
DEV void drain() { asm volatile("s_waitcnt vmcnt(0)" ::: "memory"); }
DEV u32x4 ld4(rsrc_t r, u32 off) { return __builtin_amdgcn_raw_buffer_load_b128(r, off, 0, SC1); }
DEV void st4(rsrc_t r, u32 off, u32x4 v) { __builtin_amdgcn_raw_buffer_store_b128(v, r, off, 0, SC1); }
DEV u32 ldw(const char* ws, u32 off) { return __hip_atomic_load((const u32*)(ws + off), __ATOMIC_RELAXED, __HIP_MEMORY_SCOPE_AGENT); }
DEV void stw(char* ws, u32 off, u32 v) { __hip_atomic_store((u32*)(ws + off), v, __ATOMIC_RELAXED, __HIP_MEMORY_SCOPE_AGENT); }
DEV void addw(char* ws, u32 off) { __hip_atomic_fetch_add((u32*)(ws + off), 1u, __ATOMIC_RELAXED, __HIP_MEMORY_SCOPE_AGENT); }
template <int SLEEP> DEV u32 wait_ge(const char* ws, u32 off, u32 target) {
  u32 v = 0;
#pragma unroll 1
  for (int n = 0; n < SPIN_CAP; ++n) {
    v = __builtin_amdgcn_readfirstlane(ldw(ws, off));
    if (v >= target) break;
    __builtin_amdgcn_s_sleep(SLEEP);
  }
  asm volatile("" ::: "memory");
  return v;
}
template <int SLEEP> DEV void wait_wg(const char* ws, u32 off, u32 target, int w, bool last = false) {
  if (w == 0) { if (last) wait_ge<1>(ws, off, target); else wait_ge<SLEEP>(ws, off, target); }
  lds_bar();
}
DEV f32x4 zv() { f32x4 z = {0, 0, 0, 0}; asm volatile("" : "+v"(z)); return z; }
template <class T> DEV void pin_acc(T& v) { asm volatile("" : "+a"(v)); }
template <int NK, bool ACC> DEV void load_w2(u32x4 (&w)[NK][2], const u16* base, u32 kstep, u32 rstride, u32 loff) {
  constexpr int DEPTH = 7;
  u32 t[DEPTH + 1][8];
#pragma unroll
  for (int ks = 0; ks < NK + DEPTH; ++ks) {
    if (ks < NK) {
#pragma unroll
      for (int j = 0; j < 8; ++j) t[ks % (DEPTH + 1)][j] = *(const u32*)(base + (size_t)(ks * kstep + j * rstride) + loff);
    }
    SB;
    if (ks >= DEPTH) {
      const int k = ks - DEPTH;
#pragma unroll
      for (int p = 0; p < 4; ++p) {
        w[k][0][p] = ilo(t[k % (DEPTH + 1)][2 * p], t[k % (DEPTH + 1)][2 * p + 1]); w[k][1][p] = ihi(t[k % (DEPTH + 1)][2 * p], t[k % (DEPTH + 1)][2 * p + 1]);
      }
      if (ACC) { pin_acc(w[k][0]); pin_acc(w[k][1]); }
      SB;
    }
  }
}
DEV u32 dec(u32 lo, u32 hi, float inv) {
  const f32x2 v = __builtin_elementwise_fma(f32x2{(float)(int)lo, (float)(int)hi}, f32x2{inv, inv}, f32x2{976.f * inv, 0.f});
  return pk(v[0], v[1]);
}
DEV int poll_macc(rsrc_t r, u32 off, u32x4 (&q)[4]) {
  int n;
  for (n = 0; n < SPIN_CAP; ++n) {
    asm volatile("" ::: "memory");
#pragma unroll
    for (int i = 0; i < 4; ++i) q[i] = ld4(r, off + i * 2048);
    u32 m = 32;
#pragma unroll
    for (int i = 0; i < 4; ++i) m &= q[i][0] & q[i][2];
    if (__all(m != 0)) break;
  }
  return n;
}

__global__ __launch_bounds__(256) void setup_kernel(const u16* c, const u16* wmod, const u16* wv, const u16* wo, char* ws) {
  __shared__ float red[32 * 64];
  __shared__ float sc[512];
  const int b = blockIdx.x, tid = threadIdx.x;
  for (u32 i = b * 256 + tid; i < ZERO_BYTES / 16; i += gridDim.x * 256) ((u32x4*)ws)[i] = u32x4{0, 0, 0, 0};
  float* mod = (float*)(ws + OFF_MOD);
  float* nrm = (float*)(ws + OFF_NRM);
  if (b < 48) {
    u32x4 wr[16];
#pragma unroll
    for (int it = 0; it < 16; ++it) wr[it] = *(const u32x4*)(wmod + (size_t)(it * 32 + (tid >> 3)) * 3072 + b * 64 + (tid & 7) * 8);
    for (int i = tid; i < 512; i += 256) { float v = bflo(c[i]); sc[i] = rbf(v / (1.f + __expf(-v))); }
    __syncthreads();
    float acc[8] = {0, 0, 0, 0, 0, 0, 0, 0};
#pragma unroll
    for (int it = 0; it < 16; ++it) {
      float s = sc[it * 32 + (tid >> 3)];
#pragma unroll
      for (int p = 0; p < 4; ++p) { acc[2 * p] += s * bflo(wr[it][p]); acc[2 * p + 1] += s * bfhi(wr[it][p]); }
    }
#pragma unroll
    for (int j = 0; j < 8; ++j) red[(tid >> 3) * 64 + (tid & 7) * 8 + j] = acc[j];
    __syncthreads();
    if (tid < 64) { float s = 0; for (int i = 0; i < 32; ++i) s += red[i * 64 + tid]; mod[b * 64 + tid] = rbf(s); }
  } else {
    const int h = (b - 48) >> 3, part = (b - 48) & 7;
    u32x4 v[4], o[4];
#pragma unroll
    for (int i = 0; i < 4; ++i) {
      v[i] = *(const u32x4*)(wv + (size_t)(part * 64 + i * 16 + (tid >> 4)) * 4096 + h * 128 + (tid & 15) * 8);
      o[i] = *(const u32x4*)(wo + (size_t)(h * 128 + i * 32 + (tid >> 3)) * 512 + part * 64 + (tid & 7) * 8);
    }
    float a = 0, cs[8] = {0, 0, 0, 0, 0, 0, 0, 0};
#pragma unroll
    for (int i = 0; i < 4; ++i)
#pragma unroll
      for (int p = 0; p < 4; ++p) {
        a += bflo(v[i][p]) * bflo(v[i][p]) + bfhi(v[i][p]) * bfhi(v[i][p]);
        cs[2 * p] += bflo(o[i][p]) * bflo(o[i][p]); cs[2 * p + 1] += bfhi(o[i][p]) * bfhi(o[i][p]);
      }
#pragma unroll
    for (int j = 0; j < 8; ++j) red[(tid >> 3) * 64 + (tid & 7) * 8 + j] = cs[j];
    sc[tid] = a;
    __syncthreads();
    if (tid < 64) {
      float s = 0; for (int i = 0; i < 32; ++i) s += red[i * 64 + tid];
      float t = sc[tid] + sc[tid + 64] + sc[tid + 128] + sc[tid + 192];
      s = xmax(max16(s)); t = xsum(sum16(t));
      if (tid == 0) { nrm[h * 8 + part] = t; nrm[256 + h * 8 + part] = s; }
    }
  }
}

#define HOP_WAIT drain()
DEV void chain_role(const Ptrs& P, char* lds, rsrc_t rw, int h, int lane, int w) {
  char* L_WV = lds + w * 32768;
  char* L_M = lds + 131072;
  char* L_Q = L_M + 8192;
  char* L_K = L_Q + 2304;
  char* L_O = L_K + 2304;
  const int c = lane & 15, g = lane >> 4;
  const int sidx = (lane & 8) ? 0xEEEE : 0x4444;
  const float* mod = (const float*)(P.ws + OFF_MOD);
  const float* nrm = (const float*)(P.ws + OFF_NRM);
  u32x8 wqk[4][8], wo[8][2];
  const int rot = (h + (h >> 3)) & 3, qs = ((h + (h >> 3)) >> 2) & 1;
  {
    const u32 loff = (32 * (g >> 1) + 2 * (g & 1)) * 4096;
    const u16* vb = P.wv + h * 128 + 32 * w;
    const u16* wb = (w < 2 ? P.wq : P.wk) + h * 128 + (w & 1) * 64;
    u32 tv[4][16];
    u32x2 tq[4][16];
#pragma unroll
    for (int s = 0; s < 19; ++s) {
#pragma unroll
      for (int p = 0; p < 16; ++p) {
        const int row = 128 * ((s & 7) >> 1) + 4 * (s & 1) + 64 * (p >> 3) + 16 * ((p >> 1) & 1) + 8 * ((p >> 2) & 1) + (p & 1);
        if (s < 8) tv[s & 3][p] = *(const u32*)(vb + (size_t)row * 4096 + (loff + 2 * c));
        else if (s < 16) tq[s & 3][p] = *(const u32x2*)(wb + (size_t)row * 4096 + (loff + 4 * c));
      }
      SB;
      const int k = s - 3;
      if (k >= 0 && k < 8) {
        u32x8 f0, f1;
#pragma unroll
        for (int p = 0; p < 8; ++p) { f0[p] = ilo(tv[k & 3][2 * p], tv[k & 3][2 * p + 1]); f1[p] = ihi(tv[k & 3][2 * p], tv[k & 3][2 * p + 1]); }
        *(u32x4*)(L_WV + ((k * 2 + 0) * 64 + lane) * 16) = f0.lo; *(u32x4*)(L_WV + ((k * 2 + 1) * 64 + lane) * 16) = f0.hi;
        *(u32x4*)(L_WV + ((16 + k * 2 + 0) * 64 + lane) * 16) = f1.lo; *(u32x4*)(L_WV + ((16 + k * 2 + 1) * 64 + lane) * 16) = f1.hi;
      } else if (k >= 8) {
#pragma unroll
        for (int p = 0; p < 8; ++p) {
          const u32x2 r0 = tq[k & 3][2 * p], r1 = tq[k & 3][2 * p + 1];
          wqk[0][k - 8][p] = ilo(r0[0], r1[0]); wqk[1][k - 8][p] = ihi(r0[0], r1[0]);
          wqk[2][k - 8][p] = ilo(r0[1], r1[1]); wqk[3][k - 8][p] = ihi(r0[1], r1[1]);
        }
#pragma unroll
        for (int t = 0; t < 4; ++t) pin_acc(wqk[t][k - 8]);
      }
      SB;
    }
    const u16* ob = P.wo + (size_t)h * 128 * 512 + 128 * w;
    const u32 ooff = (16 * (g >> 1) + 4 * (g & 1)) * 512 + 8 * c;
    u32x4 to[32];
#pragma unroll
    for (int i = 0; i < 32; ++i) to[i] = *(const u32x4*)(ob + (64 * (i >> 4) + 32 * ((i >> 3) & 1) + 8 * ((i >> 1) & 1) + 2 * ((i >> 2) & 1) + (i & 1)) * 512 + ooff);
    SB;
#pragma unroll
    for (int i = 0; i < 16; ++i)
#pragma unroll
      for (int u = 0; u < 4; ++u) {
        const u32 r0 = qs ? to[2 * i][u ^ 2] : to[2 * i][u], r1 = qs ? to[2 * i + 1][u ^ 2] : to[2 * i + 1][u];
        wo[2 * u][i >> 3][i & 7] = ilo(r0, r1); wo[2 * u + 1][i >> 3][i & 7] = ihi(r0, r1);
      }
  }
  const lptr kxr = lfix(lds + 147456 + lane * 16), kxw = lfix(lds + 147456 + w * 2048 + lane * 16);
  LWR(kxw + 8192) = wo[7][1].lo; LWR(kxw + 9216) = wo[7][1].hi;
  float gb, hx2;
  {
    float a = 0, b = 0;
#pragma unroll
    for (int i = 0; i < 8; ++i) { a += nrm[(lane & 31) * 8 + i]; b = fmaxf(b, nrm[256 + (lane & 31) * 8 + i]); }
    gb = 0.5f * 1.03f * xsum(sum16(sqrtf(a * b)));
    float s2 = 0, cm = 0;
#pragma unroll
    for (int i = 0; i < 8; ++i) { float sa = mod[lane * 8 + i]; s2 += sa * sa; cm = fmaxf(cm, fabsf(1.f + mod[512 + lane * 8 + i])); }
    float hx = 1.01f * (22.6275f * xmax(max16(cm)) + sqrtf(xsum(sum16(s2))));
    hx2 = hx * hx;
  }
  const int mtok = lane >> 3, msub = lane & 7;
  const lptr mw = lfix(L_M + (2 * w * 64 + msub * 8 + (mtok ^ msub)) * 16);
  const lptr mr = lfix(L_M + ((lane >> 3) * 8 + ((lane & 7) ^ (lane >> 3))) * 16);
  {
    const u16* a = P.mem0 + mtok * 512 + 128 * w + 16 * msub;
#pragma unroll
    for (int f = 0; f < 2; ++f) {
      u32x2 x0 = *(const u32x2*)(a + 4 * f), x1 = *(const u32x2*)(a + 4 * f + 8);
      LWR(mw + f * 1024) = u32x4{x0[0], x1[0], x0[1], x1[1]};
    }
  }
  const u32 kvf = OFF_KVF + h * 256;
  u32x4 ks[2], vx[2];
  STAMP(1);
  wait_ge<2>(P.ws, kvf, 1);
  STAMP(2);
#pragma unroll
  for (int a = 0; a < 2; ++a) { ks[a] = ld4(rw, OFF_KV + h * 16384 + (2 * w + a) * 1024 + lane * 16); vx[a] = ld4(rw, OFF_KV + h * 16384 + 8192 + (2 * w + a) * 1024 + lane * 16); }
  LWR(kxw) = ks[0]; LWR(kxw + 1024) = ks[1];
  u32 fl = ldw(P.ws, kvf + 4), kb = OFF_KV + h * 16384 + 2 * w * 1024 + lane * 16, fs = __builtin_amdgcn_readfirstlane(fl);
  const lptr qkw = lfix((w < 2 ? L_Q : L_K) + (8 * (w & 1) + (c >> 1)) * 144 + 64 * (g & 1) + 4 * (c & 1) + 8 * (lane >> 5));
  const lptr qkr = lfix(L_Q + g * 144 + (lane & 7) * 16);
  const lptr wvr = lfix(L_WV + lane * 16);
  const lptr ow = lfix(L_O + ((4 * w + g) * 8 + (lane & 7)) * 16), orr = lfix(L_O + (lane ^ rot) * 16);
  u32 aoff[4];
#pragma unroll
  for (int r = 0; r < 4; ++r) { aoff[r] = (w * 32 + (lane >> 5) * 8 + 4 * (g & 1) + (r ^ rot)) * 256 + c * 8; asm volatile("" : "+v"(aoff[r])); }
  const u32 poff = (w * 32 + mtok) * 256 + msub * 16;
  lds_bar();

  f32x4 aq[4] = {zv(), zv(), zv(), zv()}, av[2] = {zv(), zv()};
  const u32x2 onem = {g < 2 ? 0x3f803f80u : 0u, g < 2 ? 0x3f803f80u : 0u};
  for (int s = 0; s < NSEG - 1; ++s) {
    STAMP(64 + s); CYC(5);
    f32x4 gm, sm, s0, s1;
    u32x4 mf[8];
    u32x8 wb[2][2];
#define LDV(k) _Pragma("unroll") for (int a = 0; a < 2; ++a) { wb[(k) & 1][a].lo = LRD(wvr + (a * 16 + (k) * 2) * 1024); wb[(k) & 1][a].hi = LRD(wvr + (a * 16 + (k) * 2 + 1) * 1024); }
#define MMV(k) { smld(av[0], mf[k], wb[(k) & 1][0], sidx); smld(av[1], mf[k], wb[(k) & 1][1], sidx); }
#define MQK(k) _Pragma("unroll") for (int t = 0; t < 4; ++t) aq[t] = smma(mf[k], wqk[t][k], aq[t], sidx);
#pragma unroll
    for (int k = 0; k < 8; ++k) mf[k] = LRD(mr + k * 1024);
    LDV(0);
    SB;
    if (fs == 0) wait_ge<2>(P.ws, kvf + 4 * (s + 1), 1);
    fl = ldw(P.ws, kvf + 4 * (s + 2));
    SB;
    MQK(0); SB; LDV(1); SB;
    MQK(1); mld0(gm, mf[0], mf[0]); mld(gm, mf[1], mf[1]); SB;
    MQK(2); mld(gm, mf[2], mf[2]); mld(gm, mf[3], mf[3]); mld(gm, mf[4], mf[4]); SB;
    MMV(0); SB; LDV(2); mld(gm, mf[5], mf[5]); mld(gm, mf[6], mf[6]); mld(gm, mf[7], mf[7]); SB;
    MQK(3); SB;
    MMV(1); SB; LDV(3); SB;
    asm volatile("" : "+v"(gm));
    int n2 = max(max(__float_as_int(gm[0]), __float_as_int(gm[1])), max(__float_as_int(gm[2]), __float_as_int(gm[3])));
#define MQ1(k, t) SB; aq[t] = smma(mf[k], wqk[t][k], aq[t], sidx); SB;
    MQ1(4, 0); n2 = imaxd<0xB1>(n2); MQ1(4, 1); n2 = imaxd<0x4E>(n2); MQ1(4, 2); n2 = imaxd<0x141>(n2); MQ1(4, 3); n2 = imaxd<0x128>(n2);
    MQ1(5, 0); auto p16 = __builtin_amdgcn_permlane16_swap((u32)n2, (u32)n2, false, false);
    MQ1(5, 1); n2 = max((int)p16[0], (int)p16[1]);
    MQ1(5, 2); auto p32 = __builtin_amdgcn_permlane32_swap((u32)n2, (u32)n2, false, false);
    MQ1(5, 3); float inv = fmaxf(2.f * __int_as_float(max((int)p32[0], (int)p32[1])), hx2);
    MQ1(6, 0); inv = __builtin_amdgcn_sqrtf(inv);
    MQ1(6, 1); inv = fmaxf(inv * gb, 1e-20f) * (1.f / 2080374784.f);
    MQ1(6, 2); kb += 32 * 16384;
    MQ1(6, 3);
    MQK(7); SB;
    MMV(2); SB;
    LDV(4);
    const u32 e0 = pk(swapadd(aq[0][0], aq[1][0]), swapadd(aq[2][0], aq[3][0])), e1 = pk(swapadd(aq[0][1], aq[1][1]), swapadd(aq[2][1], aq[3][1]));
    const u32 e2 = pk(swapadd(aq[0][2], aq[1][2]), swapadd(aq[2][2], aq[3][2])), e3 = pk(swapadd(aq[0][3], aq[1][3]), swapadd(aq[2][3], aq[3][3]));
    LW1(qkw) = e0; LW1(qkw + 16) = e1; LW1(qkw + 32) = e2; LW1(qkw + 48) = e3;
    SB;
    MMV(3); SB;
    u32x4 qf[4], kf[4], kx[8];
    LDV(5); kx[0] = LRD(kxr); kx[4] = LRD(kxr + 4096);
    SB;
    asm volatile("s_waitcnt lgkmcnt(6)\n\ts_barrier" ::: "memory");
    CYC(0);
    qf[0] = LRD(qkr); kf[0] = LRD(qkr + 2304); qf[1] = LRD(qkr + 576); kf[1] = LRD(qkr + 2304 + 576);
    SB;
    MMV(4); SB;
    kx[1] = LRD(kxr + 1024); kx[5] = LRD(kxr + 5120); qf[2] = LRD(qkr + 1152); kf[2] = LRD(qkr + 2304 + 1152);
    SB;
    MMV(5); SB;
    kx[2] = LRD(kxr + 2048); kx[6] = LRD(kxr + 6144); qf[3] = LRD(qkr + 1728); kf[3] = LRD(qkr + 2304 + 1728);
    SB;
    mld0(s0, kx[0], qf[0]); mld0(s1, kx[4], qf[0]); mld0(sm, kf[0], qf[0]);
    SB; kx[3] = LRD(kxr + 3072); kx[7] = LRD(kxr + 7168); SB;
    mld(s0, kx[1], qf[1]); mld(s1, kx[5], qf[1]); mld(sm, kf[1], qf[1]);
    mld(s0, kx[2], qf[2]); mld(s1, kx[6], qf[2]); mld(sm, kf[2], qf[2]);
    SB; LDV(6); SB;
    mld(s0, kx[3], qf[3]); mld(s1, kx[7], qf[3]); mld(sm, kf[3], qf[3]);
    SB; LDV(7); SB;
    u32x2 o1, o2;
    asm volatile("v_mov_b64 %0, %5\n\tv_mov_b64 %1, %5\n\ts_nop 1" : "=v"(o1), "=v"(o2), "+v"(s0), "+v"(s1), "+v"(sm) : "s"(0x3f803f803f803f80ull));
    const u32x4 one = {o1[0], o1[1], o2[0], o2[1]};
    float mx = fmax2(fmax3(fmax3(s0[0], s0[1], s0[2]), fmax3(s0[3], s1[0], s1[1]), fmax3(s1[2], s1[3], sm[0])), fmax3(sm[1], sm[2], sm[3]));
    mx = xmax(mx) * SM_C;
    SB; MMV(6); SB;
#pragma unroll
    for (int r = 0; r < 4; ++r) s0[r] = __builtin_amdgcn_exp2f(fmaf(s0[r], SM_C, -mx));
    SB; MMV(7); SB;
#pragma unroll
    for (int r = 0; r < 4; ++r) s1[r] = __builtin_amdgcn_exp2f(fmaf(s1[r], SM_C, -mx));
    u32x4 px = {pk(s0[0], s0[1]), pk(s0[2], s0[3]), pk(s1[0], s1[1]), pk(s1[2], s1[3])};
    f32x4 o[2], rsum;
    SB;
    mvv0(rsum, one, px); mld0(o[0], vx[0], px); mld0(o[1], vx[1], px);
    SB;
#pragma unroll
    for (int a = 0; a < 2; ++a) { ks[a] = ld4(rw, kb + a * 1024); vx[a] = __builtin_amdgcn_raw_buffer_load_b128(rw, kb + a * 1024, 8192, SC1); }
    SB;
    const u32x2 vm0 = {pk(av[0][0], av[0][1]), pk(av[0][2], av[0][3])}, vm1 = {pk(av[1][0], av[1][1]), pk(av[1][2], av[1][3])};
#pragma unroll
    for (int r = 0; r < 4; ++r) sm[r] = __builtin_amdgcn_exp2f(fmaf(sm[r], SM_C, -mx));
    u32x2 pm = {pk(sm[0], sm[1]), pk(sm[2], sm[3])};
    SB;
    m16(rsum, onem, pm);
    asm("v_mfma_f32_16x16x16_bf16 %0, %2, %4, %0\n\tv_mfma_f32_16x16x16_bf16 %1, %3, %4, %1" : "+v"(o[0]), "+v"(o[1]) : "v"(vm0), "v"(vm1), "v"(pm));
    SB;
    asm volatile("s_nop 4" : "+v"(rsum));
    float rs = __builtin_amdgcn_rcpf(rsum[0] * inv);
    asm volatile("s_nop 0" : "+v"(o[0]), "+v"(o[1]), "+v"(rs));
    if (!(lane & 8))
      LWR(ow) = u32x4{pk(o[0][0] * rs, o[1][0] * rs), pk(o[0][1] * rs, o[1][1] * rs), pk(o[0][2] * rs, o[1][2] * rs), pk(o[0][3] * rs, o[1][3] * rs)};
    lds_bar();
    CYC(1);
    u32x4 of0 = LRD(orr), of1 = LRD(orr + 1024);
    wo[7][1].lo = LRD(kxw + 8192); wo[7][1].hi = LRD(kxw + 9216);
    SB;
    f32x4 t[8] = {zv(), zv(), zv(), zv(), zv(), zv(), zv(), zv()};
    char* ab[2] = {P.ws + OFF_MACC + (s + 1) * 32768 + qs * 4096, P.ws + OFF_MACC + (s + 1) * 32768 + (qs ^ 1) * 4096};
    SB;
#define ENC(q, r) { u32 lo = ((u32)(int)swapadd(t[4 * q][r], t[4 * q + 2][r]) & ~63u) | 1u, hi = (u32)(int)swapadd(t[4 * q + 1][r], t[4 * q + 3][r]); \
      asm volatile("global_atomic_add_x2 %0, %1, %2" :: "v"(aoff[r]), "v"(u32x2{lo, hi}), "s"(ab[q]) : "memory"); }
#pragma unroll
    for (int i = 0; i < 4; ++i) smld(t[i], of0, wo[i][0], sidx);
#pragma unroll
    for (int i = 0; i < 4; ++i) smld(t[i], of1, wo[i][1], sidx);
    SB;
#pragma unroll
    for (int r = 0; r < 4; ++r) {
      smld(t[4 + (2 * r & 3)], r < 2 ? of0 : of1, wo[4 + (2 * r & 3)][r >> 1], sidx);
      smld(t[5 + (2 * r & 3)], r < 2 ? of0 : of1, wo[5 + (2 * r & 3)][r >> 1], sidx);
      if (r == 0) asm volatile("s_nop 5" : "+v"(t[0]), "+v"(t[1]), "+v"(t[2]), "+v"(t[3]));
      SB;
      ENC(0, r);
      SB;
    }
    asm volatile("" : "+v"(t[4]), "+v"(t[5]), "+v"(t[6]), "+v"(t[7]));
#pragma unroll
    for (int r = 0; r < 4; ++r) ENC(1, r);
    CYC(2);
#pragma unroll
    for (int i = 0; i < 4; ++i) aq[i] = zv();
    av[0] = zv(); av[1] = zv();
    HOP_WAIT;
    CYC(3);
    u32x4 q[4];
    const int rounds = poll_macc(rw, OFF_MACC + (s + 1) * 32768 + poff, q);
    CYC(4);
    drain();
    fs = __builtin_amdgcn_readfirstlane(fl);
    LWR(kxw) = ks[0]; LWR(kxw + 1024) = ks[1];
#pragma unroll
    for (int f = 0; f < 2; ++f)
      LWR(mw + f * 1024) = u32x4{dec(q[2 * f][0], q[2 * f][1], inv), dec(q[2 * f][2], q[2 * f][3], inv), dec(q[2 * f + 1][0], q[2 * f + 1][1], inv), dec(q[2 * f + 1][2], q[2 * f + 1][3], inv)};
    lds_bar();
    if (h == 0 && w == 0 && lane == 0) stw(P.ws, OFF_RDY + (s + 1) * 128, __float_as_uint(inv));
  }
}

DEV void proj_role(const Ptrs& P, char* lds, rsrc_t rw, int h, int r0, int tid, int lane, int w) {
  char* L_H = lds;
  char* L_S = lds + 32768;
  char* L_MT = lds + 81920;
  char* L_KM = lds + 98304;
  const int c = lane & 15, g = lane >> 4;
  u32x4 wq[16][2], wk[16][2], wv[16][2];
  const u32 loff = g * 8 * 4096 + h * 128 + 32 * w + 2 * c;
  load_w2<16, true>(wk, P.wk, 32 * 4096, 4096, loff);
  load_w2<16, true>(wv, P.wv, 32 * 4096, 4096, loff);
  load_w2<16, false>(wq, P.wq, 32 * 4096, 4096, loff);
#pragma unroll
  for (int i = 0; i < 4; ++i) *(u32x4*)(L_MT + (i * 256 + tid) * 16) = u32x4{0, 0, 0, 0};
  for (int i = 0; i <= 16; ++i) {
    if (i < 16) {
      const int s = 4 * i + r0;
      char* slot = L_S + (i & 1) * 24576;
      STAMP(64 + i * 4);
      wait_wg<8>(P.ws, OFF_HF + s * 128, 1, w);
      u32x4 t[8];
#pragma unroll
      for (int j = 0; j < 8; ++j) t[j] = ld4(rw, OFF_H + s * 32768 + (j * 256 + tid) * 16);
#pragma unroll
      for (int j = 0; j < 8; ++j) *(u32x4*)(L_H + (j * 256 + tid) * 16) = t[j];
      lds_bar();
      const u32 kb = OFF_KV + (s * 32 + h) * 16384 + lane * 16;
      u32x2 vp[2][2];
#pragma unroll
      for (int mt = 0; mt < 2; ++mt) {
        f32x4 ak[2], av[2];
#pragma unroll
        for (int ks = 0; ks < 16; ++ks) {
          u32x4 hf = *(const u32x4*)(L_H + ((mt * 16 + ks) * 64 + lane) * 16);
#pragma unroll
          for (int a = 0; a < 2; ++a) {
            if (ks == 0) { mav0(ak[a], wk[ks][a], hf); mva0(av[a], hf, wv[ks][a]); } else { mav(ak[a], wk[ks][a], hf); mva(av[a], hf, wv[ks][a]); }
          }
        }
        fin(ak[0], ak[1], av[0], av[1]);
        u32x4 kf = {pk(ak[0][0], ak[0][1]), pk(ak[0][2], ak[0][3]), pk(ak[1][0], ak[1][1]), pk(ak[1][2], ak[1][3])};
        st4(rw, kb + (mt * 4 + w) * 1024, kf);
        *(u32x4*)(slot + 8192 + ((mt * 4 + w) * 64 + lane) * 16) = kf;
#pragma unroll
        for (int a = 0; a < 2; ++a) vp[mt][a] = u32x2{pk(av[a][0], av[a][1]), pk(av[a][2], av[a][3])};
      }
#pragma unroll
      for (int a = 0; a < 2; ++a) {
        u32x4 vf = {vp[0][a][0], vp[0][a][1], vp[1][a][0], vp[1][a][1]};
        st4(rw, kb + 8192 + (2 * w + a) * 1024, vf);
        *(u32x4*)(slot + 16384 + ((2 * w + a) * 64 + lane) * 16) = vf;
      }
#pragma unroll
      for (int mt = 0; mt < 2; ++mt) {
        f32x4 aq[2];
#pragma unroll
        for (int ks = 0; ks < 16; ++ks) {
          u32x4 hf = *(const u32x4*)(L_H + ((mt * 16 + ks) * 64 + lane) * 16);
#pragma unroll
          for (int a = 0; a < 2; ++a) { if (ks == 0) mvv0(aq[a], wq[ks][a], hf); else mvv(aq[a], wq[ks][a], hf); }
        }
        fin(aq[0], aq[1]);
        *(u32x4*)(slot + ((mt * 4 + w) * 64 + lane) * 16) = u32x4{pk(aq[0][0], aq[0][1]), pk(aq[0][2], aq[0][3]), pk(aq[1][0], aq[1][1]), pk(aq[1][2], aq[1][3])};
      }
      drain();
      lds_bar();
      if (tid == 0) stw(P.ws, OFF_KVF + h * 256 + s * 4, 1u);
      STAMP(64 + i * 4 + 1);
    }
    if (i >= 1) {
      const int s = 4 * (i - 1) + r0;
      const char* slot = L_S + ((i - 1) & 1) * 24576 + lane * 16;
      f32x4 x00, x01, x11;
#pragma unroll
      for (int k = 0; k < 4; ++k) {
        u32x4 q0 = *(const u32x4*)(slot + k * 1024), q1 = *(const u32x4*)(slot + (4 + k) * 1024);
        u32x4 k0 = *(const u32x4*)(slot + (8 + k) * 1024), k1 = *(const u32x4*)(slot + (12 + k) * 1024);
        if (k == 0) { mvv0(x00, k0, q0); mvv0(x01, k0, q1); mvv0(x11, k1, q1); } else { mvv(x00, k0, q0); mvv(x01, k0, q1); mvv(x11, k1, q1); }
      }
      fin(x00, x01, x11);
      const int tok = lane >> 3, sub = lane & 7;
      if (s == 0) {
#pragma unroll
        for (int j = 0; j < 2; ++j) {
          const int u = j * 256 + tid, t = u >> 6, cg = u & 63;
          *(u32x4*)(L_MT + ((cg >> 2) * 64 + t + 16 * (cg & 3)) * 16) = *(const u32x4*)(P.mem0 + t * 512 + cg * 8);
        }
      } else {
        wait_wg<12>(P.ws, OFF_RDY + s * 128, 1, w, s == NSEG - 1);
        const float inv = __uint_as_float(ldw(P.ws, OFF_RDY + s * 128));
        STAMP(64 + (i - 1) * 4 + 2);
        u32x4 q[4];
        poll_macc(rw, OFF_MACC + s * 32768 + (w * 32 + tok) * 256 + sub * 16, q);
#pragma unroll
        for (int u = 0; u < 4; ++u)
#pragma unroll
          for (int e = 0; e < 2; ++e)
            *(u32*)(L_MT + ((4 * w + (sub >> 1)) * 64 + tok + 16 * ((2 * sub + e) & 3)) * 16 + 4 * u) = dec(q[u][2 * e], q[u][2 * e + 1], inv);
      }
      lds_bar();
      f32x4 ak[2], av[2];
#pragma unroll
      for (int ks = 0; ks < 16; ++ks) {
        u32x4 mf = *(const u32x4*)(L_MT + (ks * 64 + lane) * 16);
#pragma unroll
        for (int a = 0; a < 2; ++a) {
          if (ks == 0) { mav0(ak[a], wk[ks][a], mf); mva0(av[a], mf, wv[ks][a]); } else { mav(ak[a], wk[ks][a], mf); mva(av[a], mf, wv[ks][a]); }
        }
      }
      fin(ak[0], ak[1], av[0], av[1]);
      *(u32x4*)(L_KM + (w * 64 + lane) * 16) = u32x4{pk(ak[0][0], ak[0][1]), pk(ak[0][2], ak[0][3]), pk(ak[1][0], ak[1][1]), pk(ak[1][2], ak[1][3])};
      lds_bar();
      f32x4 m0, m1;
#pragma unroll
      for (int k = 0; k < 4; ++k) {
        u32x4 km = *(const u32x4*)(L_KM + (k * 64 + lane) * 16);
        u32x4 q0 = *(const u32x4*)(slot + k * 1024), q1 = *(const u32x4*)(slot + (4 + k) * 1024);
        if (k == 0) { mvv0(m0, km, q0); mvv0(m1, km, q1); } else { mvv(m0, km, q0); mvv(m1, km, q1); }
      }
      fin(m0, m1);
      const float NEG = -__builtin_inff();
#pragma unroll
      for (int r = 0; r < 4; ++r) {
        if (g >= 2) { m0[r] = NEG; m1[r] = NEG; }
        if (4 * g + r > c) { x00[r] = NEG; x11[r] = NEG; }
      }
      float a0 = xmax(fmaxf(fmaxf(fmaxf(m0[0], m0[1]), fmaxf(m0[2], m0[3])), fmaxf(fmaxf(x00[0], x00[1]), fmaxf(x00[2], x00[3])))) * SM_C;
      float a1 = xmax(fmaxf(fmaxf(fmaxf(fmaxf(m1[0], m1[1]), fmaxf(m1[2], m1[3])), fmaxf(fmaxf(x01[0], x01[1]), fmaxf(x01[2], x01[3]))),
                            fmaxf(fmaxf(x11[0], x11[1]), fmaxf(x11[2], x11[3])))) * SM_C;
      float t0 = 0, t1 = 0;
#pragma unroll
      for (int r = 0; r < 4; ++r) {
        m0[r] = __builtin_amdgcn_exp2f(fmaf(m0[r], SM_C, -a0)); x00[r] = __builtin_amdgcn_exp2f(fmaf(x00[r], SM_C, -a0));
        m1[r] = __builtin_amdgcn_exp2f(fmaf(m1[r], SM_C, -a1)); x01[r] = __builtin_amdgcn_exp2f(fmaf(x01[r], SM_C, -a1)); x11[r] = __builtin_amdgcn_exp2f(fmaf(x11[r], SM_C, -a1));
        t0 += m0[r] + x00[r]; t1 += m1[r] + x01[r] + x11[r];
      }
      t0 = __builtin_amdgcn_rcpf(xsum(t0)); t1 = __builtin_amdgcn_rcpf(xsum(t1));
      u32x2 pm0 = {pk(m0[0] * t0, m0[1] * t0), pk(m0[2] * t0, m0[3] * t0)}, pm1 = {pk(m1[0] * t1, m1[1] * t1), pk(m1[2] * t1, m1[3] * t1)};
      u32x4 px0 = {pk(x00[0] * t0, x00[1] * t0), pk(x00[2] * t0, x00[3] * t0), 0u, 0u};
      u32x4 px1 = {pk(x01[0] * t1, x01[1] * t1), pk(x01[2] * t1, x01[3] * t1), pk(x11[0] * t1, x11[1] * t1), pk(x11[2] * t1, x11[3] * t1)};
      f32x4 o0[2], o1[2];
#pragma unroll
      for (int a = 0; a < 2; ++a) {
        u32x4 vf = *(const u32x4*)(slot + (16 + 2 * w + a) * 1024);
        u32x2 vm = {pk(av[a][0], av[a][1]), pk(av[a][2], av[a][3])};
        mvv0(o0[a], vf, px0); m16(o0[a], vm, pm0);
        mvv0(o1[a], vf, px1); m16(o1[a], vm, pm1);
      }
      fin(o0[0], o0[1], o1[0], o1[1]);
      const u32 ob = OFF_O + (s * 256 + 4 * h + w) * 1024 + lane * 16;
      st4(rw, ob, u32x4{pk(o0[0][0], o0[1][0]), pk(o0[0][1], o0[1][1]), pk(o0[0][2], o0[1][2]), pk(o0[0][3], o0[1][3])});
      st4(rw, ob + 131072, u32x4{pk(o1[0][0], o1[1][0]), pk(o1[0][1], o1[1][1]), pk(o1[0][2], o1[1][2]), pk(o1[0][3], o1[1][3])});
      drain();
      lds_bar();
      if (tid == 0) addw(P.ws, OFF_OC + s * 128);
      STAMP(64 + (i - 1) * 4 + 3);
    }
  }
}

DEV void oproj_role(const Ptrs& P, char* lds, rsrc_t rw, int n, int th, int lane, int w) {
  const int c = lane & 15, g = lane >> 4;
  const float* mod = (const float*)(P.ws + OFF_MOD);
  u32x4 wo[32][2];
  load_w2<32, true>(wo, P.wo, 32 * 512, 512, (w * 1024 + g * 8) * 512 + 32 * n + 2 * c);
  float ga[8];
#pragma unroll
  for (int j = 0; j < 8; ++j) ga[j] = mod[1024 + 32 * n + 8 * g + j];
  for (int s = 0; s < NSEG; ++s) {
    const u32 row = (s * 32 + th * 16 + c) * 512 + 32 * n + 8 * g;
    u32x4 xr = *(const u32x4*)(P.x + row);
    wait_wg<12>(P.ws, OFF_OC + s * 128, 32, w, s == NSEG - 1);
    STAMP(64 + s * 2);
    u32x4 of[32];
#pragma unroll
    for (int k = 0; k < 32; ++k) of[k] = ld4(rw, OFF_O + ((s * 2 + th) * 128 + 32 * w + k) * 1024 + lane * 16);
    SB;
    f32x4 a0, a1;
#pragma unroll
    for (int k = 0; k < 32; ++k) {
      if (k == 0) { mav0(a0, wo[k][0], of[k]); mav0(a1, wo[k][1], of[k]); } else { mav(a0, wo[k][0], of[k]); mav(a1, wo[k][1], of[k]); }
    }
    fin(a0, a1);
    char* red = lds + (s & 1) * 8192;
    *(f32x4*)(red + (w * 128 + lane) * 16) = a0; *(f32x4*)(red + (w * 128 + 64 + lane) * 16) = a1;
    lds_bar();
    STAMP(64 + s * 2 + 1);
    if (w == (s & 3)) {
      a0 = f32x4{0, 0, 0, 0}; a1 = a0;
#pragma unroll
      for (int i = 0; i < 4; ++i) { a0 += *(const f32x4*)(red + (i * 128 + lane) * 16); a1 += *(const f32x4*)(red + (i * 128 + 64 + lane) * 16); }
      u32x4 y;
#pragma unroll
      for (int r = 0; r < 4; ++r) y[r] = pk(bflo(xr[r]) + rbf(ga[2 * r] * rbf(a0[r])), bfhi(xr[r]) + rbf(ga[2 * r + 1] * rbf(a1[r])));
      st4(rw, OFF_X1 + row * 2, y);
      drain();
      if (lane == 0) addw(P.ws, OFF_X1C + (s * 2 + th) * 128);
    }
  }
}

template <int NCH, int LPR> DEV void ln_coef(u32x4 (&sh)[NCH], u32x4 (&sc)[NCH], const float* shift, const float* scl, int part) {
#pragma unroll
  for (int i = 0; i < NCH; ++i)
#pragma unroll
    for (int p = 0; p < 4; ++p) {
      const int d = (i * LPR + part) * 8 + 2 * p;
      const f32x2 a = *(const f32x2*)(shift + d), b = *(const f32x2*)(scl + d);
      sh[i][p] = pk(a[0], a[1]); sc[i][p] = pk(1.f + b[0], 1.f + b[1]);
    }
}
template <int NCH, int LPR> DEV void ln_mod(u32x4 (&v)[NCH], const u32x4 (&sh)[NCH], const u32x4 (&sc)[NCH]) {
  float sum = 0;
#pragma unroll
  for (int i = 0; i < NCH; ++i)
#pragma unroll
    for (int p = 0; p < 4; ++p) sum += bflo(v[i][p]) + bfhi(v[i][p]);
  const float mean = (LPR == 8 ? sum8(sum) : sum16(sum)) * (1.f / 512.f);
  float var = 0;
#pragma unroll
  for (int i = 0; i < NCH; ++i)
#pragma unroll
    for (int p = 0; p < 4; ++p) { float a = bflo(v[i][p]) - mean, b = bfhi(v[i][p]) - mean; var += a * a + b * b; }
  const float rstd = 1.f / sqrtf((LPR == 8 ? sum8(var) : sum16(var)) * (1.f / 512.f) + 1e-5f);
#pragma unroll
  for (int i = 0; i < NCH; ++i)
#pragma unroll
    for (int p = 0; p < 4; ++p)
      v[i][p] = pk(fmaf((bflo(v[i][p]) - mean) * rstd, bflo(sc[i][p]), bflo(sh[i][p])), fmaf((bfhi(v[i][p]) - mean) * rstd, bfhi(sc[i][p]), bfhi(sh[i][p])));
}

DEV void ln1_task(const Ptrs& P, rsrc_t rw, int s, int tid) {
  const float* mod = (const float*)(P.ws + OFF_MOD);
  const int tok = tid >> 3, part = tid & 7;
  u32x4 v[8];
#pragma unroll
  for (int i = 0; i < 8; ++i) v[i] = *(const u32x4*)(P.x + (s * 32 + tok) * 512 + (i * 8 + part) * 8);
  u32x4 sh[8], sc[8];
  ln_coef<8, 8>(sh, sc, mod, mod + 512, part);
  ln_mod<8, 8>(v, sh, sc);
#pragma unroll
  for (int i = 0; i < 8; ++i) st4(rw, OFF_H + s * 32768 + (((tok >> 4) * 16 + 2 * i + (part >> 2)) * 64 + (tok & 15) + 16 * (part & 3)) * 16, v[i]);
  drain();
  lds_bar();
  if (tid == 0) stw(P.ws, OFF_HF + s * 128, 1u);
  STAMP(3);
}

DEV void ffn1_role(const Ptrs& P, char* lds, rsrc_t rw, int fg, int th, int par, int tid, int lane, int w) {
  const int c = lane & 15, g = lane >> 4, kf = min(4 * fg + w, NKF - 1);
  const bool act = 4 * fg + w < NKF;
  const float* mod = (const float*)(P.ws + OFF_MOD);
  u32x4 w1[16][2];
  load_w2<16, false>(w1, P.w1, 32 * FF, FF, g * 8 * FF + 32 * kf + 2 * c);
  const u32x4 bb = *(const u32x4*)(P.b1 + 32 * kf + 8 * g);
  const int tok = tid >> 4, part = tid & 15;
  u32x4 sh[4], sc[4];
  ln_coef<4, 16>(sh, sc, mod + 1536, mod + 2048, part);
  for (int s = par; s < NSEG; s += 2) {
    wait_wg<12>(P.ws, OFF_X1C + (s * 2 + th) * 128, 16, w, s == NSEG - 1);
    STAMP(64 + s * 2);
    u32x4 v[4];
#pragma unroll
    for (int i = 0; i < 4; ++i) v[i] = ld4(rw, OFF_X1 + ((s * 32 + th * 16 + tok) * 512 + (i * 16 + part) * 8) * 2);
    ln_mod<4, 16>(v, sh, sc);
    char* lh = lds;
#pragma unroll
    for (int i = 0; i < 4; ++i) *(u32x4*)(lh + ((4 * i + (part >> 2)) * 64 + (tok ^ (part >> 2)) + 16 * (part & 3)) * 16) = v[i];
    lds_bar();
    f32x4 a0 = {}, a1 = {};
    u32x4 hf[16];
#pragma unroll
    for (int k = 0; k < 16; ++k) hf[k] = *(const u32x4*)(lh + (k * 64 + (lane ^ (k & 3))) * 16);
    SB;
#pragma unroll
    for (int k = 0; k < 16; ++k) { a0 = mma(w1[k][0], hf[k], a0); a1 = mma(w1[k][1], hf[k], a1); }
    u32x4 y;
#pragma unroll
    for (int r = 0; r < 4; ++r) {
      float u[2] = {rbf(rbf(a0[r]) + bflo(bb[r])), rbf(rbf(a1[r]) + bfhi(bb[r]))};
#pragma unroll
      for (int e = 0; e < 2; ++e) {
        float z = 1.5957691216057308f * (u[e] + 0.044715f * u[e] * u[e] * u[e]);
        u[e] = u[e] * __builtin_amdgcn_rcpf(1.f + __expf(-z));
      }
      y[r] = pk(u[0], u[1]);
    }
    if (act) st4(rw, OFF_G + ((s * 2 + th) * NKF + kf) * 1024 + lane * 16, y);
    drain();
    lds_bar();
    if (tid == 0) addw(P.ws, OFF_GC + (s * 2 + th) * 128);
    STAMP(64 + s * 2 + 1);
  }
}

DEV void ffn2_role(const Ptrs& P, char* lds, rsrc_t rw, int n, int th, int lane, int w) {
  const int c = lane & 15, g = lane >> 4;
  const float* mod = (const float*)(P.ws + OFF_MOD);
  u32x4 w2[11][4];
#pragma unroll
  for (int i = 0; i < 11; ++i) {
    const int kf = min(w + 4 * i, NKF - 1);
    u32x2 t[8];
#pragma unroll
    for (int j = 0; j < 8; ++j) t[j] = *(const u32x2*)(P.w2 + (size_t)(32 * kf + 8 * g + j) * 512 + 64 * n + 4 * c);
#pragma unroll
    for (int p = 0; p < 4; ++p) {
      w2[i][0][p] = ilo(t[2 * p][0], t[2 * p + 1][0]); w2[i][1][p] = ihi(t[2 * p][0], t[2 * p + 1][0]);
      w2[i][2][p] = ilo(t[2 * p][1], t[2 * p + 1][1]); w2[i][3][p] = ihi(t[2 * p][1], t[2 * p + 1][1]);
    }
  }
  const int col = 64 * n + 16 * g + 4 * w;
  const f32x4 gbv = *(const f32x4*)(mod + 2560 + col);
  const u32x2 b2 = *(const u32x2*)(P.b2 + col);
  for (int s = 0; s < NSEG; ++s) {
    wait_wg<12>(P.ws, OFF_GC + (s * 2 + th) * 128, 11, w, s == NSEG - 1);
    STAMP(64 + s * 2);
    u32x4 gf[11];
#pragma unroll
    for (int i = 0; i < 11; ++i) gf[i] = ld4(rw, OFF_G + ((s * 2 + th) * NKF + min(w + 4 * i, NKF - 1)) * 1024 + lane * 16);
    const u32 row = (s * 32 + th * 16 + c) * 512 + col;
    const u32x2 x1 = __builtin_amdgcn_raw_buffer_load_b64(rw, OFF_X1 + row * 2, 0, SC1);
    SB;
    if (w == 3) gf[10] = u32x4{0, 0, 0, 0};
    f32x4 acc[4] = {};
#pragma unroll
    for (int i = 0; i < 11; ++i)
#pragma unroll
      for (int t = 0; t < 4; ++t) acc[t] = mma(w2[i][t], gf[i], acc[t]);
    char* red = lds + (s & 1) * 16384;
#pragma unroll
    for (int r = 0; r < 4; ++r) *(f32x4*)(red + ((w * 4 + r) * 64 + lane) * 16) = f32x4{acc[0][r], acc[1][r], acc[2][r], acc[3][r]};
    lds_bar();
    f32x4 y = {};
#pragma unroll
    for (int i = 0; i < 4; ++i) y += *(const f32x4*)(red + ((i * 4 + w) * 64 + lane) * 16);
    u32x2 o;
#pragma unroll
    for (int p = 0; p < 2; ++p)
      o[p] = pk(bflo(x1[p]) + rbf(gbv[2 * p] * rbf(rbf(y[2 * p]) + bflo(b2[p]))), bfhi(x1[p]) + rbf(gbv[2 * p + 1] * rbf(rbf(y[2 * p + 1]) + bfhi(b2[p]))));
    *(u32x2*)(P.out + row) = o;
    STAMP(64 + s * 2 + 1);
  }
}

__global__ __launch_bounds__(256, 1) void main_kernel(const u16* x, const u16* mem0, const u16* wq, const u16* wk, const u16* wv, const u16* wo,
                                                      const u16* w1, const u16* b1, const u16* w2, const u16* b2, u16* out, char* ws) {
  __shared__ __attribute__((aligned(16))) char lds[LDS_BYTES];
  const Ptrs P = {x, mem0, wq, wk, wv, wo, w1, b1, w2, b2, out, ws};
  const int b = blockIdx.x, tid = threadIdx.x, lane = tid & 63, w = __builtin_amdgcn_readfirstlane(tid >> 6);
  const rsrc_t rw = __builtin_amdgcn_make_buffer_rsrc(ws, 0, WS_BYTES, 0x00020000);
  STAMP(0);
  if (b < 32) {
    chain_role(P, lds, rw, b, lane, w);
  } else if (b < 160) {
    proj_role(P, lds, rw, (b - 32) & 31, (b - 32) >> 5, tid, lane, w);
  } else if (b < 192) {
    oproj_role(P, lds, rw, (b - 160) >> 1, b & 1, lane, w);
  } else {
    ln1_task(P, rw, b - 192, tid);
    if (b < 208) ffn2_role(P, lds, rw, (b - 192) >> 1, b & 1, lane, w);
    else if (b < 252) ffn1_role(P, lds, rw, ((b - 208) % 22) >> 1, b & 1, (b - 208) / 22, tid, lane, w);
  }
}

extern "C" void kernel_launch(void* const* d_in, const int* in_sizes, int n_in, void* d_out, int out_size, void* d_ws, size_t ws_size,
                              hipStream_t stream) {
  const u16* const* in = (const u16* const*)d_in;
  setup_kernel<<<304, 256, 0, stream>>>(in[12], in[13], in[4], in[5], (char*)d_ws);
  main_kernel<<<256, 256, 0, stream>>>(in[0], in[1], in[2], in[3], in[4], in[5], in[8], in[9], in[10], in[11], (u16*)d_out, (char*)d_ws);
}
```

```cpp
#include <hip/hip_runtime.h>
#include <hip/hip_bf16.h>

typedef unsigned u32;
typedef unsigned long long u64;
typedef unsigned short u16;
typedef u32 u32x2 __attribute__((ext_vector_type(2)));
typedef u32 u32x4 __attribute__((ext_vector_type(4)));
typedef u32 u32x8 __attribute__((ext_vector_type(8)));
typedef float f32x4 __attribute__((ext_vector_type(4)));
typedef float f32x2 __attribute__((ext_vector_type(2)));
typedef short s16x4 __attribute__((ext_vector_type(4)));
typedef __bf16 bf16x2 __attribute__((ext_vector_type(2)));
typedef __bf16 bf16x8 __attribute__((ext_vector_type(8)));
typedef __bf16 bf16x16 __attribute__((ext_vector_type(16)));
typedef __amdgpu_buffer_rsrc_t rsrc_t;
#define DEV __device__ __forceinline__

constexpr int NSEG = 64, DM = 512, FF = 1376, NKF = 43;
constexpr u32 OFF_MACC = 0;
constexpr u32 OFF_KVF = 2u << 20;
constexpr u32 OFF_HF = OFF_KVF + 8192;
constexpr u32 OFF_RDY = OFF_HF + 8192;
constexpr u32 OFF_OC = OFF_RDY + 8192;
constexpr u32 OFF_X1C = OFF_OC + 8192;
constexpr u32 OFF_GC = OFF_X1C + 16384;
constexpr u32 ZERO_BYTES = OFF_GC + 16384;
constexpr u32 OFF_MOD = ZERO_BYTES;
constexpr u32 OFF_NRM = OFF_MOD + 12288;
constexpr u32 OFF_H = 3u << 20;
constexpr u32 OFF_KV = 5u << 20;
constexpr u32 OFF_O = 37u << 20;
constexpr u32 OFF_X1 = 53u << 20;
constexpr u32 OFF_G = 55u << 20;
constexpr u32 WS_BYTES = 61u << 20;
constexpr int LDS_BYTES = 163840;
constexpr int SPIN_CAP = 1 << 17;
constexpr int SC1 = 16;
constexpr float SM_C = 0.08838834764831845f * 1.4426950408889634f;

#define STAMP(id)
#define CYC(id)
struct Ptrs {
  const u16 *x, *mem0, *wq, *wk, *wv, *wo, *w1, *b1, *w2, *b2;
  u16* out;
  char* ws;
};

DEV float bflo(u32 v) { return __uint_as_float(v << 16); }
DEV float bfhi(u32 v) { return __uint_as_float(v & 0xffff0000u); }
DEV u32 pk(float a, float b) { bf16x2 v = {(__bf16)a, (__bf16)b}; return __builtin_bit_cast(u32, v); }
DEV float rbf(float a) { return (float)(__bf16)a; }
DEV u32 ilo(u32 a, u32 b) { return __builtin_amdgcn_perm(b, a, 0x05040100u); }
DEV u32 ihi(u32 a, u32 b) { return __builtin_amdgcn_perm(b, a, 0x07060302u); }
DEV f32x4 mma(u32x4 a, u32x4 b, f32x4 c) {
  return __builtin_amdgcn_mfma_f32_16x16x32_bf16(__builtin_bit_cast(bf16x8, a), __builtin_bit_cast(bf16x8, b), c, 0, 0, 0);
}
#define MMA_ASM(NAME, PRE, OP, TA, CA, CB) \
  DEV void NAME(f32x4& c, TA a, TA b) { asm(PRE OP " %0, %1, %2, %0" : "+v"(c) : CA(a), CB(b)); } \
  DEV void NAME##0(f32x4& c, TA a, TA b) { asm(PRE OP " %0, %1, %2, 0" : "=&v"(c) : CA(a), CB(b)); }
MMA_ASM(mvv, "s_nop 1\n\t", "v_mfma_f32_16x16x32_bf16", u32x4, "v", "v")
MMA_ASM(m16, "s_nop 1\n\t", "v_mfma_f32_16x16x16_bf16", u32x2, "v", "v")
MMA_ASM(mld, "", "v_mfma_f32_16x16x32_bf16", u32x4, "v", "v")
MMA_ASM(mav, "", "v_mfma_f32_16x16x32_bf16", u32x4, "a", "v")
MMA_ASM(mva, "", "v_mfma_f32_16x16x32_bf16", u32x4, "v", "a")
DEV void fin(f32x4& a) { asm volatile("s_nop 7" : "+v"(a)); }
DEV void fin(f32x4& a, f32x4& b) { asm volatile("s_nop 7" : "+v"(a), "+v"(b)); }
DEV void fin(f32x4& a, f32x4& b, f32x4& c) { asm volatile("s_nop 7" : "+v"(a), "+v"(b), "+v"(c)); }
DEV void fin(f32x4& a, f32x4& b, f32x4& c, f32x4& d) { asm volatile("s_nop 7" : "+v"(a), "+v"(b), "+v"(c), "+v"(d)); }
typedef __attribute__((address_space(3))) char* lptr;
DEV lptr lfix(char* p) { lptr q = (lptr)p; asm volatile("" : "+v"(q)); return q; }
#define LRD(p) (*(const __attribute__((address_space(3))) u32x4*)(p))
#define LWR(p) (*(__attribute__((address_space(3))) u32x4*)(p))
#define LW1(p) (*(__attribute__((address_space(3))) u32*)(p))
DEV f32x4 smma(u32x4 a, u32x8 b, f32x4 c, int idx) {
  return __builtin_amdgcn_smfmac_f32_16x16x64_bf16(__builtin_bit_cast(bf16x8, a), __builtin_bit_cast(bf16x16, b), c, idx, 0, 0);
}
DEV void smld(f32x4& c, u32x4 a, u32x8 b, int idx) {
  asm volatile("v_smfmac_f32_16x16x64_bf16 %0, %1, %2, %3" : "+v"(c) : "v"(a), "v"(b), "v"(idx));
}
DEV float swapadd(float a, float b) {
  auto t = __builtin_amdgcn_permlane32_swap(__float_as_uint(a), __float_as_uint(b), false, false);
  float r;
  asm("v_add_f32 %0, %1, %2" : "=v"(r) : "v"(t[0]), "v"(t[1]));
  return r;
}
DEV float fmax2(float a, float b) { float r; asm("v_max_f32 %0, %1, %2" : "=v"(r) : "v"(a), "v"(b)); return r; }
DEV float fmax3(float a, float b, float c) { float r; asm("v_max3_f32 %0, %1, %2, %3" : "=v"(r) : "v"(a), "v"(b), "v"(c)); return r; }
DEV float xmax(float x) {
  auto s = __builtin_amdgcn_permlane16_swap(__float_as_uint(x), __float_as_uint(x), false, false);
  x = fmax2(__uint_as_float(s[0]), __uint_as_float(s[1]));
  auto t = __builtin_amdgcn_permlane32_swap(__float_as_uint(x), __float_as_uint(x), false, false);
  return fmax2(__uint_as_float(t[0]), __uint_as_float(t[1]));
}
DEV int ixmax(int x) {
  auto s = __builtin_amdgcn_permlane16_swap((u32)x, (u32)x, false, false);
  x = max((int)s[0], (int)s[1]);
  auto t = __builtin_amdgcn_permlane32_swap((u32)x, (u32)x, false, false);
  return max((int)t[0], (int)t[1]);
}
template <int C> DEV int imaxd(int x) { return max(x, __builtin_amdgcn_update_dpp(x, x, C, 0xf, 0xf, true)); }
DEV int imax16(int x) { return imaxd<0x128>(imaxd<0x141>(imaxd<0x4E>(imaxd<0xB1>(x)))); }
DEV float xsum(float x) {
  auto s = __builtin_amdgcn_permlane16_swap(__float_as_uint(x), __float_as_uint(x), false, false);
  x = __uint_as_float(s[0]) + __uint_as_float(s[1]);
  auto t = __builtin_amdgcn_permlane32_swap(__float_as_uint(x), __float_as_uint(x), false, false);
  return __uint_as_float(t[0]) + __uint_as_float(t[1]);
}
template <int C> DEV float dpp(float x) { return __uint_as_float(__builtin_amdgcn_update_dpp(0, __float_as_uint(x), C, 0xf, 0xf, true)); }
DEV float sum8(float x) { x += dpp<0xB1>(x); x += dpp<0x4E>(x); return x + dpp<0x141>(x); }
DEV float sum16(float x) { x = sum8(x); return x + dpp<0x128>(x); }
DEV float max16(float x) { x = fmaxf(x, dpp<0xB1>(x)); x = fmaxf(x, dpp<0x4E>(x)); x = fmaxf(x, dpp<0x141>(x)); return fmaxf(x, dpp<0x128>(x)); }
DEV void lds_bar() { asm volatile("s_waitcnt lgkmcnt(0)\n\ts_barrier" ::: "memory"); }
#define SB __builtin_amdgcn_sched_barrier(0)
DEV void drain() { asm volatile("s_waitcnt vmcnt(0)" ::: "memory"); }
DEV u32x4 ld4(rsrc_t r, u32 off) { return __builtin_amdgcn_raw_buffer_load_b128(r, off, 0, SC1); }
DEV void st4(rsrc_t r, u32 off, u32x4 v) { __builtin_amdgcn_raw_buffer_store_b128(v, r, off, 0, SC1); }
DEV u32 ldw(const char* ws, u32 off) { return __hip_atomic_load((const u32*)(ws + off), __ATOMIC_RELAXED, __HIP_MEMORY_SCOPE_AGENT); }
DEV void stw(char* ws, u32 off, u32 v) { __hip_atomic_store((u32*)(ws + off), v, __ATOMIC_RELAXED, __HIP_MEMORY_SCOPE_AGENT); }
DEV void addw(char* ws, u32 off) { __hip_atomic_fetch_add((u32*)(ws + off), 1u, __ATOMIC_RELAXED, __HIP_MEMORY_SCOPE_AGENT); }
template <int SLEEP> DEV u32 wait_ge(const char* ws, u32 off, u32 target) {
  u32 v = 0;
#pragma unroll 1
  for (int n = 0; n < SPIN_CAP; ++n) {
    v = __builtin_amdgcn_readfirstlane(ldw(ws, off));
    if (v >= target) break;
    __builtin_amdgcn_s_sleep(SLEEP);
  }
  asm volatile("" ::: "memory");
  return v;
}
template <int SLEEP> DEV void wait_wg(const char* ws, u32 off, u32 target, int w, bool last = false) {
  if (w == 0) { if (last) wait_ge<1>(ws, off, target); else wait_ge<SLEEP>(ws, off, target); }
  lds_bar();
}
DEV f32x4 zv() { f32x4 z = {0, 0, 0, 0}; asm volatile("" : "+v"(z)); return z; }
DEV rsrc_t wrs(const u16* p) { return __builtin_amdgcn_make_buffer_rsrc((void*)p, 0, 0x7fffffff, 0x00020000); }
template <class T> DEV void pin_acc(T& v) { asm volatile("" : "+a"(v)); }
template <int NK, bool ACC> DEV void load_w2(u32x4 (&w)[NK][2], const u16* base, u32 kstep, u32 rstride, u32 loff) {
  constexpr int DEPTH = 7;
  u32 t[DEPTH + 1][8];
  const rsrc_t r = wrs(base);
#pragma unroll
  for (int ks = 0; ks < NK + DEPTH; ++ks) {
    if (ks < NK) {
#pragma unroll
      for (int j = 0; j < 8; ++j) t[ks % (DEPTH + 1)][j] = __builtin_amdgcn_raw_buffer_load_b32(r, loff * 2, (ks * kstep + j * rstride) * 2, 0);
    }
    SB;
    if (ks >= DEPTH) {
      const int k = ks - DEPTH;
#pragma unroll
      for (int p = 0; p < 4; ++p) {
        w[k][0][p] = ilo(t[k % (DEPTH + 1)][2 * p], t[k % (DEPTH + 1)][2 * p + 1]); w[k][1][p] = ihi(t[k % (DEPTH + 1)][2 * p], t[k % (DEPTH + 1)][2 * p + 1]);
      }
      if (ACC) { pin_acc(w[k][0]); pin_acc(w[k][1]); }
      SB;
    }
  }
}
DEV u32 dec(u32 lo, u32 hi, float inv) {
  const f32x2 v = __builtin_elementwise_fma(f32x2{(float)(int)lo, (float)(int)hi}, f32x2{inv, inv}, f32x2{976.f * inv, 0.f});
  return pk(v[0], v[1]);
}
DEV int poll_macc(rsrc_t r, u32 off, u32x4 (&q)[4]) {
  int n;
  for (n = 0; n < SPIN_CAP; ++n) {
    asm volatile("" ::: "memory");
#pragma unroll
    for (int i = 0; i < 4; ++i) q[i] = ld4(r, off + i * 2048);
    u32 m = 32;
#pragma unroll
    for (int i = 0; i < 4; ++i) m &= q[i][0] & q[i][2];
    if (__all(m != 0)) break;
  }
  return n;
}

__global__ __launch_bounds__(256) void setup_kernel(const u16* c, const u16* wmod, const u16* wv, const u16* wo, char* ws) {
  __shared__ float red[32 * 64];
  __shared__ float sc[512];
  const int b = blockIdx.x, tid = threadIdx.x;
  for (u32 i = b * 256 + tid; i < ZERO_BYTES / 16; i += gridDim.x * 256) ((u32x4*)ws)[i] = u32x4{0, 0, 0, 0};
  float* mod = (float*)(ws + OFF_MOD);
  float* nrm = (float*)(ws + OFF_NRM);
  if (b < 48) {
    u32x4 wr[16];
#pragma unroll
    for (int it = 0; it < 16; ++it) wr[it] = *(const u32x4*)(wmod + (size_t)(it * 32 + (tid >> 3)) * 3072 + b * 64 + (tid & 7) * 8);
    for (int i = tid; i < 512; i += 256) { float v = bflo(c[i]); sc[i] = rbf(v / (1.f + __expf(-v))); }
    __syncthreads();
    float acc[8] = {0, 0, 0, 0, 0, 0, 0, 0};
#pragma unroll
    for (int it = 0; it < 16; ++it) {
      float s = sc[it * 32 + (tid >> 3)];
#pragma unroll
      for (int p = 0; p < 4; ++p) { acc[2 * p] += s * bflo(wr[it][p]); acc[2 * p + 1] += s * bfhi(wr[it][p]); }
    }
#pragma unroll
    for (int j = 0; j < 8; ++j) red[(tid >> 3) * 64 + (tid & 7) * 8 + j] = acc[j];
    __syncthreads();
    if (tid < 64) { float s = 0; for (int i = 0; i < 32; ++i) s += red[i * 64 + tid]; mod[b * 64 + tid] = rbf(s); }
  } else {
    const int h = (b - 48) >> 3, part = (b - 48) & 7;
    u32x4 v[4], o[4];
#pragma unroll
    for (int i = 0; i < 4; ++i) {
      v[i] = *(const u32x4*)(wv + (size_t)(part * 64 + i * 16 + (tid >> 4)) * 4096 + h * 128 + (tid & 15) * 8);
      o[i] = *(const u32x4*)(wo + (size_t)(h * 128 + i * 32 + (tid >> 3)) * 512 + part * 64 + (tid & 7) * 8);
    }
    float a = 0, cs[8] = {0, 0, 0, 0, 0, 0, 0, 0};
#pragma unroll
    for (int i = 0; i < 4; ++i)
#pragma unroll
      for (int p = 0; p < 4; ++p) {
        a += bflo(v[i][p]) * bflo(v[i][p]) + bfhi(v[i][p]) * bfhi(v[i][p]);
        cs[2 * p] += bflo(o[i][p]) * bflo(o[i][p]); cs[2 * p + 1] += bfhi(o[i][p]) * bfhi(o[i][p]);
      }
#pragma unroll
    for (int j = 0; j < 8; ++j) red[(tid >> 3) * 64 + (tid & 7) * 8 + j] = cs[j];
    sc[tid] = a;
    __syncthreads();
    if (tid < 64) {
      float s = 0; for (int i = 0; i < 32; ++i) s += red[i * 64 + tid];
      float t = sc[tid] + sc[tid + 64] + sc[tid + 128] + sc[tid + 192];
      s = xmax(max16(s)); t = xsum(sum16(t));
      if (tid == 0) { nrm[h * 8 + part] = t; nrm[256 + h * 8 + part] = s; }
    }
  }
}

DEV void chain_role(const Ptrs& P, char* lds, rsrc_t rw, int h, int lane, int w) {
  char* L_WV = lds + w * 32768;
  char* L_M = lds + 131072;
  char* L_Q = L_M + 8192;
  char* L_K = L_Q + 2304;
  char* L_O = L_K + 2304;
  const int c = lane & 15, g = lane >> 4;
  const int sidx = (lane & 8) ? 0xEEEE : 0x4444;
  const float* mod = (const float*)(P.ws + OFF_MOD);
  const float* nrm = (const float*)(P.ws + OFF_NRM);
  u32x8 wqk[4][8], wo[8][2];
  const int rot = (h + (h >> 3)) & 3;
  {
    const u32 loff = (32 * (g >> 1) + 2 * (g & 1)) * 4096;
    const rsrc_t vb = wrs(P.wv + h * 128 + (size_t)w * 128 * 4096);
    const rsrc_t wb = wrs((w < 2 ? P.wq : P.wk) + h * 128 + (w & 1) * 64);
    u32x4 tv[2][16];
    u32x2 tq[4][16];
#define WROW(s, p) (128 * ((s) >> 1) + 4 * ((s) & 1) + 64 * ((p) >> 3) + 16 * (((p) >> 1) & 1) + 8 * (((p) >> 2) & 1) + ((p) & 1))
#pragma unroll
    for (int i = 0; i < 32; ++i) tv[i >> 4][i & 15] = __builtin_amdgcn_raw_buffer_load_b128(vb, (loff + 8 * c) * 2, WROW(i >> 4, i & 15) * 8192, 0);
    SB;
#pragma unroll
    for (int s = 0; s < 11; ++s) {
      if (s < 8) {
#pragma unroll
        for (int p = 0; p < 16; ++p) tq[s & 3][p] = __builtin_amdgcn_raw_buffer_load_b64(wb, (loff + 4 * c) * 2, WROW(s, p) * 8192, 0);
      }
      SB;
      if (s < 2) {
        char* dst = lds + (c >> 2) * 32768 + ((2 * w + s) * 128 + 16 * g + 4 * (c & 3)) * 16;
#pragma unroll
        for (int u = 0; u < 4; ++u) {
          u32x8 f0, f1;
#pragma unroll
          for (int p = 0; p < 8; ++p) { f0[p] = ilo(tv[s][2 * p][u], tv[s][2 * p + 1][u]); f1[p] = ihi(tv[s][2 * p][u], tv[s][2 * p + 1][u]); }
          *(u32x4*)(dst + u * 16) = f0.lo; *(u32x4*)(dst + u * 16 + 1024) = f0.hi;
          *(u32x4*)(dst + u * 16 + 16384) = f1.lo; *(u32x4*)(dst + u * 16 + 17408) = f1.hi;
        }
      } else if (s >= 3) {
        const int k = s - 3;
#pragma unroll
        for (int p = 0; p < 8; ++p) {
          const u32x2 r0 = tq[k & 3][2 * p], r1 = tq[k & 3][2 * p + 1];
          wqk[0][k][p] = ilo(r0[0], r1[0]); wqk[1][k][p] = ihi(r0[0], r1[0]);
          wqk[2][k][p] = ilo(r0[1], r1[1]); wqk[3][k][p] = ihi(r0[1], r1[1]);
        }
#pragma unroll
        for (int t = 0; t < 4; ++t) pin_acc(wqk[t][k]);
      }
      SB;
    }
    const rsrc_t ob = wrs(P.wo + (size_t)h * 128 * 512 + 128 * w);
    const u32 ooff = (16 * (g >> 1) + 4 * (g & 1)) * 512 + 8 * c;
    u32x4 to[32];
#pragma unroll
    for (int i = 0; i < 32; ++i) to[i] = __builtin_amdgcn_raw_buffer_load_b128(ob, ooff * 2, (64 * (i >> 4) + 32 * ((i >> 3) & 1) + 8 * ((i >> 1) & 1) + 2 * ((i >> 2) & 1) + (i & 1)) * 1024, 0);
    SB;
#pragma unroll
    for (int i = 0; i < 16; ++i)
#pragma unroll
      for (int u = 0; u < 4; ++u) {
        const u32 r0 = to[2 * i][u], r1 = to[2 * i + 1][u];
        wo[2 * u][i >> 3][i & 7] = ilo(r0, r1); wo[2 * u + 1][i >> 3][i & 7] = ihi(r0, r1);
      }
  }
  const lptr kxr = lfix(lds + 147456 + lane * 16), kxw = lfix(lds + 147456 + w * 2048 + lane * 16);
  LWR(kxw + 8192) = wo[7][1].lo; LWR(kxw + 9216) = wo[7][1].hi;
  float gb, hx2;
  {
    float a = 0, b = 0;
#pragma unroll
    for (int i = 0; i < 8; ++i) { a += nrm[(lane & 31) * 8 + i]; b = fmaxf(b, nrm[256 + (lane & 31) * 8 + i]); }
    gb = 0.5f * 1.03f * xsum(sum16(sqrtf(a * b)));
    float s2 = 0, cm = 0;
#pragma unroll
    for (int i = 0; i < 8; ++i) { float sa = mod[lane * 8 + i]; s2 += sa * sa; cm = fmaxf(cm, fabsf(1.f + mod[512 + lane * 8 + i])); }
    float hx = 1.01f * (22.6275f * xmax(max16(cm)) + sqrtf(xsum(sum16(s2))));
    hx2 = hx * hx;
  }
  const int mtok = lane >> 3, msub = lane & 7;
  const lptr mw = lfix(L_M + (2 * w * 64 + msub * 8 + (mtok ^ msub)) * 16);
  const lptr mr = lfix(L_M + ((lane >> 3) * 8 + ((lane & 7) ^ (lane >> 3))) * 16);
  {
    const u16* a = P.mem0 + mtok * 512 + 128 * w + 16 * msub;
#pragma unroll
    for (int f = 0; f < 2; ++f) {
      u32x2 x0 = *(const u32x2*)(a + 4 * f), x1 = *(const u32x2*)(a + 4 * f + 8);
      LWR(mw + f * 1024) = u32x4{x0[0], x1[0], x0[1], x1[1]};
    }
  }
  const u32 kvf = OFF_KVF + h * 256;
  u32x4 ks[2], vx[2];
  STAMP(1);
  wait_ge<2>(P.ws, kvf, 1);
  STAMP(2);
#pragma unroll
  for (int a = 0; a < 2; ++a) { ks[a] = ld4(rw, OFF_KV + h * 16384 + (2 * w + a) * 1024 + lane * 16); vx[a] = ld4(rw, OFF_KV + h * 16384 + 8192 + (2 * w + a) * 1024 + lane * 16); }
  LWR(kxw) = ks[0]; LWR(kxw + 1024) = ks[1];
  u32 fl = ldw(P.ws, kvf + 4), kb = OFF_KV + h * 16384 + 2 * w * 1024 + lane * 16, fs = __builtin_amdgcn_readfirstlane(fl);
  const lptr qkw = lfix((w < 2 ? L_Q : L_K) + (8 * (w & 1) + (c >> 1)) * 144 + 64 * (g & 1) + 4 * (c & 1) + 8 * (lane >> 5));
  const lptr qkr = lfix(L_Q + g * 144 + (lane & 7) * 16);
  const lptr wvr = lfix(L_WV + lane * 16);
  const lptr ow = lfix(L_O + ((4 * w + g) * 8 + (lane & 7)) * 16), orr = lfix(L_O + (lane ^ rot) * 16);
  u32 aoff[4];
#pragma unroll
  for (int r = 0; r < 4; ++r) { aoff[r] = (w * 32 + (lane >> 5) * 8 + 4 * (g & 1) + (r ^ rot)) * 256 + c * 8; asm volatile("" : "+v"(aoff[r])); }
  const u32 poff = (w * 32 + mtok) * 256 + msub * 16;
  lds_bar();

  f32x4 aq[4] = {zv(), zv(), zv(), zv()}, av[2] = {zv(), zv()};
  const u32x2 onem = {g < 2 ? 0x3f803f80u : 0u, g < 2 ? 0x3f803f80u : 0u};
  for (int s = 0; s < NSEG - 1; ++s) {
    STAMP(64 + s); CYC(5);
    f32x4 gm, sm, s0, s1;
    u32x4 mf[8];
    u32x8 wb[2][2];
#define LDV(k) _Pragma("unroll") for (int a = 0; a < 2; ++a) { wb[(k) & 1][a].lo = LRD(wvr + (a * 16 + (k) * 2) * 1024); wb[(k) & 1][a].hi = LRD(wvr + (a * 16 + (k) * 2 + 1) * 1024); }
#define MMV(k) { smld(av[0], mf[k], wb[(k) & 1][0], sidx); smld(av[1], mf[k], wb[(k) & 1][1], sidx); }
#define MQK(k) _Pragma("unroll") for (int t = 0; t < 4; ++t) aq[t] = smma(mf[k], wqk[t][k], aq[t], sidx);
#pragma unroll
    for (int k = 0; k < 8; ++k) mf[k] = LRD(mr + k * 1024);
    LDV(0);
    SB;
    if (fs == 0) wait_ge<2>(P.ws, kvf + 4 * (s + 1), 1);
    fl = ldw(P.ws, kvf + 4 * (s + 2));
    SB;
    MQK(0); SB; LDV(1); SB;
    MQK(1); mld0(gm, mf[0], mf[0]); mld(gm, mf[1], mf[1]); SB;
    MQK(2); mld(gm, mf[2], mf[2]); mld(gm, mf[3], mf[3]); mld(gm, mf[4], mf[4]); SB;
    MMV(0); SB; LDV(2); mld(gm, mf[5], mf[5]); mld(gm, mf[6], mf[6]); mld(gm, mf[7], mf[7]); SB;
    MQK(3); SB;
    MMV(1); SB; LDV(3); SB;
    asm volatile("" : "+v"(gm));
    int n2 = max(max(__float_as_int(gm[0]), __float_as_int(gm[1])), max(__float_as_int(gm[2]), __float_as_int(gm[3])));
#define MQ1(k, t) SB; aq[t] = smma(mf[k], wqk[t][k], aq[t], sidx); SB;
    MQ1(4, 0); n2 = imaxd<0xB1>(n2); MQ1(4, 1); n2 = imaxd<0x4E>(n2); MQ1(4, 2); n2 = imaxd<0x141>(n2); MQ1(4, 3); n2 = imaxd<0x128>(n2);
    MQ1(5, 0); auto p16 = __builtin_amdgcn_permlane16_swap((u32)n2, (u32)n2, false, false);
    MQ1(5, 1); n2 = max((int)p16[0], (int)p16[1]);
    MQ1(5, 2); auto p32 = __builtin_amdgcn_permlane32_swap((u32)n2, (u32)n2, false, false);
    MQ1(5, 3); float inv = fmaxf(2.f * __int_as_float(max((int)p32[0], (int)p32[1])), hx2);
    MQ1(6, 0); inv = __builtin_amdgcn_sqrtf(inv);
    MQ1(6, 1); inv = fmaxf(inv * gb, 1e-20f) * (1.f / 2080374784.f);
    MQ1(6, 2); kb += 32 * 16384;
    MQ1(6, 3);
    MQK(7); SB;
    MMV(2); SB;
    LDV(4);
    const u32 e0 = pk(swapadd(aq[0][0], aq[1][0]), swapadd(aq[2][0], aq[3][0])), e1 = pk(swapadd(aq[0][1], aq[1][1]), swapadd(aq[2][1], aq[3][1]));
    const u32 e2 = pk(swapadd(aq[0][2], aq[1][2]), swapadd(aq[2][2], aq[3][2])), e3 = pk(swapadd(aq[0][3], aq[1][3]), swapadd(aq[2][3], aq[3][3]));
    LW1(qkw) = e0; LW1(qkw + 16) = e1; LW1(qkw + 32) = e2; LW1(qkw + 48) = e3;
    SB;
    MMV(3); SB;
    u32x4 qf[4], kf[4], kx[8];
    LDV(5); kx[0] = LRD(kxr); kx[4] = LRD(kxr + 4096);
    SB;
    asm volatile("s_waitcnt lgkmcnt(6)\n\ts_barrier" ::: "memory");
    CYC(0);
    qf[0] = LRD(qkr); kf[0] = LRD(qkr + 2304); qf[1] = LRD(qkr + 576); kf[1] = LRD(qkr + 2304 + 576);
    SB;
    MMV(4); SB;
    kx[1] = LRD(kxr + 1024); kx[5] = LRD(kxr + 5120); qf[2] = LRD(qkr + 1152); kf[2] = LRD(qkr + 2304 + 1152);
    SB;
    MMV(5); SB;
    kx[2] = LRD(kxr + 2048); kx[6] = LRD(kxr + 6144); qf[3] = LRD(qkr + 1728); kf[3] = LRD(qkr + 2304 + 1728);
    SB;
    mld0(s0, kx[0], qf[0]); mld0(s1, kx[4], qf[0]); mld0(sm, kf[0], qf[0]);
    SB; kx[3] = LRD(kxr + 3072); kx[7] = LRD(kxr + 7168); SB;
    mld(s0, kx[1], qf[1]); mld(s1, kx[5], qf[1]); mld(sm, kf[1], qf[1]);
    mld(s0, kx[2], qf[2]); mld(s1, kx[6], qf[2]); mld(sm, kf[2], qf[2]);
    SB; LDV(6); SB;
    mld(s0, kx[3], qf[3]); mld(s1, kx[7], qf[3]); mld(sm, kf[3], qf[3]);
    SB; LDV(7); SB;
    u32x2 o1, o2;
    asm volatile("v_mov_b64 %0, %5\n\tv_mov_b64 %1, %5\n\ts_nop 1" : "=v"(o1), "=v"(o2), "+v"(s0), "+v"(s1), "+v"(sm) : "s"(0x3f803f803f803f80ull));
    const u32x4 one = {o1[0], o1[1], o2[0], o2[1]};
    float mx = fmax2(fmax3(fmax3(s0[0], s0[1], s0[2]), fmax3(s0[3], s1[0], s1[1]), fmax3(s1[2], s1[3], sm[0])), fmax3(sm[1], sm[2], sm[3]));
    mx = xmax(mx) * SM_C;
    SB; MMV(6); SB;
#pragma unroll
    for (int r = 0; r < 4; ++r) s0[r] = __builtin_amdgcn_exp2f(fmaf(s0[r], SM_C, -mx));
    SB; MMV(7); SB;
#pragma unroll
    for (int r = 0; r < 4; ++r) s1[r] = __builtin_amdgcn_exp2f(fmaf(s1[r], SM_C, -mx));
    u32x4 px = {pk(s0[0], s0[1]), pk(s0[2], s0[3]), pk(s1[0], s1[1]), pk(s1[2], s1[3])};
    f32x4 o[2], rsum;
    SB;
    mvv0(rsum, one, px); mld0(o[0], vx[0], px); mld0(o[1], vx[1], px);
    SB;
#pragma unroll
    for (int a = 0; a < 2; ++a) { ks[a] = ld4(rw, kb + a * 1024); vx[a] = __builtin_amdgcn_raw_buffer_load_b128(rw, kb + a * 1024, 8192, SC1); }
    SB;
    const u32x2 vm0 = {pk(av[0][0], av[0][1]), pk(av[0][2], av[0][3])}, vm1 = {pk(av[1][0], av[1][1]), pk(av[1][2], av[1][3])};
#pragma unroll
    for (int r = 0; r < 4; ++r) sm[r] = __builtin_amdgcn_exp2f(fmaf(sm[r], SM_C, -mx));
    u32x2 pm = {pk(sm[0], sm[1]), pk(sm[2], sm[3])};
    SB;
    m16(rsum, onem, pm);
    asm("v_mfma_f32_16x16x16_bf16 %0, %2, %4, %0\n\tv_mfma_f32_16x16x16_bf16 %1, %3, %4, %1" : "+v"(o[0]), "+v"(o[1]) : "v"(vm0), "v"(vm1), "v"(pm));
    SB;
    asm volatile("s_nop 4" : "+v"(rsum));
    float rs = __builtin_amdgcn_rcpf(rsum[0] * inv);
    asm volatile("s_nop 0" : "+v"(o[0]), "+v"(o[1]), "+v"(rs));
    if (!(lane & 8))
      LWR(ow) = u32x4{pk(o[0][0] * rs, o[1][0] * rs), pk(o[0][1] * rs, o[1][1] * rs), pk(o[0][2] * rs, o[1][2] * rs), pk(o[0][3] * rs, o[1][3] * rs)};
    lds_bar();
    CYC(1);
    u32x4 of0 = LRD(orr), of1 = LRD(orr + 1024);
    wo[7][1].lo = LRD(kxw + 8192); wo[7][1].hi = LRD(kxw + 9216);
    SB;
    f32x4 t[8] = {zv(), zv(), zv(), zv(), zv(), zv(), zv(), zv()};
    char* ab[2] = {P.ws + OFF_MACC + (s + 1) * 32768, P.ws + OFF_MACC + (s + 1) * 32768 + 4096};
    SB;
#define ENC(q, r) { u32 lo = ((u32)(int)swapadd(t[4 * q][r], t[4 * q + 2][r]) & ~63u) | 1u, hi = (u32)(int)swapadd(t[4 * q + 1][r], t[4 * q + 3][r]); \
      asm volatile("global_atomic_add_x2 %0, %1, %2" :: "v"(aoff[r]), "v"(u32x2{lo, hi}), "s"(ab[q]) : "memory"); }
#pragma unroll
    for (int i = 0; i < 4; ++i) smld(t[i], of0, wo[i][0], sidx);
#pragma unroll
    for (int i = 0; i < 4; ++i) smld(t[i], of1, wo[i][1], sidx);
    SB;
#pragma unroll
    for (int r = 0; r < 4; ++r) {
      smld(t[4 + (2 * r & 3)], r < 2 ? of0 : of1, wo[4 + (2 * r & 3)][r >> 1], sidx);
      smld(t[5 + (2 * r & 3)], r < 2 ? of0 : of1, wo[5 + (2 * r & 3)][r >> 1], sidx);
      if (r == 0) asm volatile("s_nop 5" : "+v"(t[0]), "+v"(t[1]), "+v"(t[2]), "+v"(t[3]));
      SB;
      ENC(0, r);
      SB;
    }
    asm volatile("" : "+v"(t[4]), "+v"(t[5]), "+v"(t[6]), "+v"(t[7]));
#pragma unroll
    for (int r = 0; r < 4; ++r) ENC(1, r);
    CYC(2);
#pragma unroll
    for (int i = 0; i < 4; ++i) aq[i] = zv();
    av[0] = zv(); av[1] = zv();
    drain();
    CYC(3);
    u32x4 q[4];
    const int rounds = poll_macc(rw, OFF_MACC + (s + 1) * 32768 + poff, q);
    CYC(4);
    fs = __builtin_amdgcn_readfirstlane(fl);
    LWR(kxw) = ks[0]; LWR(kxw + 1024) = ks[1];
#pragma unroll
    for (int f = 0; f < 2; ++f)
      LWR(mw + f * 1024) = u32x4{dec(q[2 * f][0], q[2 * f][1], inv), dec(q[2 * f][2], q[2 * f][3], inv), dec(q[2 * f + 1][0], q[2 * f + 1][1], inv), dec(q[2 * f + 1][2], q[2 * f + 1][3], inv)};
    lds_bar();
    if (h == 0 && w == 0 && lane == 0) stw(P.ws, OFF_RDY + (s + 1) * 128, __float_as_uint(inv));
  }
}

DEV void proj_role(const Ptrs& P, char* lds, rsrc_t rw, int h, int r0, int tid, int lane, int w) {
  char* L_H = lds;
  char* L_S = lds + 32768;
  char* L_MT = lds + 81920;
  char* L_KM = lds + 98304;
  const int c = lane & 15, g = lane >> 4;
  u32x4 wq[16][2], wk[16][2], wv[16][2];
  const u32 loff = g * 8 * 4096 + h * 128 + 32 * w + 2 * c;
  load_w2<16, true>(wk, P.wk, 32 * 4096, 4096, loff);
  load_w2<16, true>(wv, P.wv, 32 * 4096, 4096, loff);
#pragma unroll
  for (int i = 0; i < 4; ++i) *(u32x4*)(L_MT + (i * 256 + tid) * 16) = u32x4{0, 0, 0, 0};
  auto kv_part = [&](int s, char* slot) __attribute__((always_inline)) {
    wait_wg<8>(P.ws, OFF_HF + s * 128, 1, w);
    u32x4 t[8];
#pragma unroll
    for (int j = 0; j < 8; ++j) t[j] = ld4(rw, OFF_H + s * 32768 + (j * 256 + tid) * 16);
#pragma unroll
    for (int j = 0; j < 8; ++j) *(u32x4*)(L_H + (j * 256 + tid) * 16) = t[j];
    lds_bar();
    const u32 kb = OFF_KV + (s * 32 + h) * 16384 + lane * 16;
    u32x2 vp[2][2];
#pragma unroll
    for (int mt = 0; mt < 2; ++mt) {
      f32x4 ak[2], av[2];
      u32x4 hfa[16];
#pragma unroll
      for (int ks = 0; ks < 16; ++ks) hfa[ks] = *(const u32x4*)(L_H + ((mt * 16 + ks) * 64 + lane) * 16);
      SB;
#pragma unroll
      for (int ks = 0; ks < 16; ++ks) {
        const u32x4 hf = hfa[ks];
#pragma unroll
        for (int a = 0; a < 2; ++a) {
          if (ks == 0) { mav0(ak[a], wk[ks][a], hf); mva0(av[a], hf, wv[ks][a]); } else { mav(ak[a], wk[ks][a], hf); mva(av[a], hf, wv[ks][a]); }
        }
      }
      fin(ak[0], ak[1], av[0], av[1]);
      u32x4 kf = {pk(ak[0][0], ak[0][1]), pk(ak[0][2], ak[0][3]), pk(ak[1][0], ak[1][1]), pk(ak[1][2], ak[1][3])};
      st4(rw, kb + (mt * 4 + w) * 1024, kf);
      *(u32x4*)(slot + 8192 + ((mt * 4 + w) * 64 + lane) * 16) = kf;
#pragma unroll
      for (int a = 0; a < 2; ++a) vp[mt][a] = u32x2{pk(av[a][0], av[a][1]), pk(av[a][2], av[a][3])};
    }
#pragma unroll
    for (int a = 0; a < 2; ++a) {
      u32x4 vf = {vp[0][a][0], vp[0][a][1], vp[1][a][0], vp[1][a][1]};
      st4(rw, kb + 8192 + (2 * w + a) * 1024, vf);
      *(u32x4*)(slot + 16384 + ((2 * w + a) * 64 + lane) * 16) = vf;
    }
  };
  kv_part(r0, L_S);
  drain();
  lds_bar();
  if (tid == 0) stw(P.ws, OFF_KVF + h * 256 + r0 * 4, 1u);
  load_w2<16, false>(wq, P.wq, 32 * 4096, 4096, loff);
  for (int i = 0; i <= 16; ++i) {
    if (i < 16) {
      const int s = 4 * i + r0;
      char* slot = L_S + (i & 1) * 24576;
      STAMP(64 + i * 4);
      if (i > 0) kv_part(s, slot);
#pragma unroll
      for (int mt = 0; mt < 2; ++mt) {
        f32x4 aq[2];
#pragma unroll
        for (int ks = 0; ks < 16; ++ks) {
          u32x4 hf = *(const u32x4*)(L_H + ((mt * 16 + ks) * 64 + lane) * 16);
#pragma unroll
          for (int a = 0; a < 2; ++a) { if (ks == 0) mld0(aq[a], wq[ks][a], hf); else mld(aq[a], wq[ks][a], hf); }
        }
        fin(aq[0], aq[1]);
        *(u32x4*)(slot + ((mt * 4 + w) * 64 + lane) * 16) = u32x4{pk(aq[0][0], aq[0][1]), pk(aq[0][2], aq[0][3]), pk(aq[1][0], aq[1][1]), pk(aq[1][2], aq[1][3])};
      }
      drain();
      lds_bar();
      if (tid == 0) stw(P.ws, OFF_KVF + h * 256 + s * 4, 1u);
      STAMP(64 + i * 4 + 1);
    }
    if (i >= 1) {
      const int s = 4 * (i - 1) + r0;
      const char* slot = L_S + ((i - 1) & 1) * 24576 + lane * 16;
      f32x4 x00, x01, x11;
#pragma unroll
      for (int k = 0; k < 4; ++k) {
        u32x4 q0 = *(const u32x4*)(slot + k * 1024), q1 = *(const u32x4*)(slot + (4 + k) * 1024);
        u32x4 k0 = *(const u32x4*)(slot + (8 + k) * 1024), k1 = *(const u32x4*)(slot + (12 + k) * 1024);
        if (k == 0) { mld0(x00, k0, q0); mld0(x01, k0, q1); mld0(x11, k1, q1); } else { mld(x00, k0, q0); mld(x01, k0, q1); mld(x11, k1, q1); }
      }
      fin(x00, x01, x11);
      const int tok = lane >> 3, sub = lane & 7;
      if (s == 0) {
#pragma unroll
        for (int j = 0; j < 2; ++j) {
          const int u = j * 256 + tid, t = u >> 6, cg = u & 63;
          *(u32x4*)(L_MT + ((cg >> 2) * 64 + t + 16 * (cg & 3)) * 16) = *(const u32x4*)(P.mem0 + t * 512 + cg * 8);
        }
      } else {
        wait_wg<12>(P.ws, OFF_RDY + s * 128, 1, w, s == NSEG - 1);
        const float inv = __uint_as_float(ldw(P.ws, OFF_RDY + s * 128));
        STAMP(64 + (i - 1) * 4 + 2);
        u32x4 q[4];
        poll_macc(rw, OFF_MACC + s * 32768 + (w * 32 + tok) * 256 + sub * 16, q);
#pragma unroll
        for (int u = 0; u < 4; ++u)
#pragma unroll
          for (int e = 0; e < 2; ++e)
            *(u32*)(L_MT + ((4 * w + (sub >> 1)) * 64 + tok + 16 * ((2 * sub + e) & 3)) * 16 + 4 * u) = dec(q[u][2 * e], q[u][2 * e + 1], inv);
      }
      lds_bar();
      f32x4 ak[2], av[2];
      u32x4 mfa[16];
#pragma unroll
      for (int ks = 0; ks < 16; ++ks) mfa[ks] = *(const u32x4*)(L_MT + (ks * 64 + lane) * 16);
      SB;
#pragma unroll
      for (int ks = 0; ks < 16; ++ks) {
        const u32x4 mf = mfa[ks];
#pragma unroll
        for (int a = 0; a < 2; ++a) {
          if (ks == 0) { mav0(ak[a], wk[ks][a], mf); mva0(av[a], mf, wv[ks][a]); } else { mav(ak[a], wk[ks][a], mf); mva(av[a], mf, wv[ks][a]); }
        }
      }
      fin(ak[0], ak[1], av[0], av[1]);
      *(u32x4*)(L_KM + (w * 64 + lane) * 16) = u32x4{pk(ak[0][0], ak[0][1]), pk(ak[0][2], ak[0][3]), pk(ak[1][0], ak[1][1]), pk(ak[1][2], ak[1][3])};
      lds_bar();
      f32x4 m0, m1;
#pragma unroll
      for (int k = 0; k < 4; ++k) {
        u32x4 km = *(const u32x4*)(L_KM + (k * 64 + lane) * 16);
        u32x4 q0 = *(const u32x4*)(slot + k * 1024), q1 = *(const u32x4*)(slot + (4 + k) * 1024);
        if (k == 0) { mld0(m0, km, q0); mld0(m1, km, q1); } else { mld(m0, km, q0); mld(m1, km, q1); }
      }
      fin(m0, m1);
      const float NEG = -__builtin_inff();
#pragma unroll
      for (int r = 0; r < 4; ++r) {
        if (g >= 2) { m0[r] = NEG; m1[r] = NEG; }
        if (4 * g + r > c) { x00[r] = NEG; x11[r] = NEG; }
      }
      float a0 = xmax(fmaxf(fmaxf(fmaxf(m0[0], m0[1]), fmaxf(m0[2], m0[3])), fmaxf(fmaxf(x00[0], x00[1]), fmaxf(x00[2], x00[3])))) * SM_C;
      float a1 = xmax(fmaxf(fmaxf(fmaxf(fmaxf(m1[0], m1[1]), fmaxf(m1[2], m1[3])), fmaxf(fmaxf(x01[0], x01[1]), fmaxf(x01[2], x01[3]))),
                            fmaxf(fmaxf(x11[0], x11[1]), fmaxf(x11[2], x11[3])))) * SM_C;
      float t0 = 0, t1 = 0;
#pragma unroll
      for (int r = 0; r < 4; ++r) {
        m0[r] = __builtin_amdgcn_exp2f(fmaf(m0[r], SM_C, -a0)); x00[r] = __builtin_amdgcn_exp2f(fmaf(x00[r], SM_C, -a0));
        m1[r] = __builtin_amdgcn_exp2f(fmaf(m1[r], SM_C, -a1)); x01[r] = __builtin_amdgcn_exp2f(fmaf(x01[r], SM_C, -a1)); x11[r] = __builtin_amdgcn_exp2f(fmaf(x11[r], SM_C, -a1));
        t0 += m0[r] + x00[r]; t1 += m1[r] + x01[r] + x11[r];
      }
      t0 = __builtin_amdgcn_rcpf(xsum(t0)); t1 = __builtin_amdgcn_rcpf(xsum(t1));
      u32x2 pm0 = {pk(m0[0] * t0, m0[1] * t0), pk(m0[2] * t0, m0[3] * t0)}, pm1 = {pk(m1[0] * t1, m1[1] * t1), pk(m1[2] * t1, m1[3] * t1)};
      u32x4 px0 = {pk(x00[0] * t0, x00[1] * t0), pk(x00[2] * t0, x00[3] * t0), 0u, 0u};
      u32x4 px1 = {pk(x01[0] * t1, x01[1] * t1), pk(x01[2] * t1, x01[3] * t1), pk(x11[0] * t1, x11[1] * t1), pk(x11[2] * t1, x11[3] * t1)};
      f32x4 o0[2], o1[2];
#pragma unroll
      for (int a = 0; a < 2; ++a) {
        u32x4 vf = *(const u32x4*)(slot + (16 + 2 * w + a) * 1024);
        mvv0(o0[a], vf, px0); mvv0(o1[a], vf, px1);
      }
      fin(o0[0], o0[1], o1[0], o1[1]);
#pragma unroll
      for (int a = 0; a < 2; ++a) {
        u32x2 vm = {pk(av[a][0], av[a][1]), pk(av[a][2], av[a][3])};
        m16(o0[a], vm, pm0); m16(o1[a], vm, pm1);
      }
      fin(o0[0], o0[1], o1[0], o1[1]);
      const u32 ob = OFF_O + (s * 256 + 4 * h + w) * 1024 + lane * 16;
      st4(rw, ob, u32x4{pk(o0[0][0], o0[1][0]), pk(o0[0][1], o0[1][1]), pk(o0[0][2], o0[1][2]), pk(o0[0][3], o0[1][3])});
      st4(rw, ob + 131072, u32x4{pk(o1[0][0], o1[1][0]), pk(o1[0][1], o1[1][1]), pk(o1[0][2], o1[1][2]), pk(o1[0][3], o1[1][3])});
      drain();
      lds_bar();
      if (tid == 0) addw(P.ws, OFF_OC + s * 128);
      STAMP(64 + (i - 1) * 4 + 3);
    }
  }
}

DEV void oproj_role(const Ptrs& P, char* lds, rsrc_t rw, int n, int th, int lane, int w) {
  const int c = lane & 15, g = lane >> 4;
  const float* mod = (const float*)(P.ws + OFF_MOD);
  u32x4 wo[32][2];
  load_w2<32, true>(wo, P.wo, 32 * 512, 512, (w * 1024 + g * 8) * 512 + 32 * n + 2 * c);
  float ga[8];
#pragma unroll
  for (int j = 0; j < 8; ++j) ga[j] = mod[1024 + 32 * n + 8 * g + j];
  for (int s = 0; s < NSEG; ++s) {
    const u32 row = (s * 32 + th * 16 + c) * 512 + 32 * n + 8 * g;
    u32x4 xr = *(const u32x4*)(P.x + row);
    wait_wg<12>(P.ws, OFF_OC + s * 128, 32, w, s == NSEG - 1);
    STAMP(64 + s * 2);
    u32x4 of[32];
#pragma unroll
    for (int k = 0; k < 32; ++k) of[k] = ld4(rw, OFF_O + ((s * 2 + th) * 128 + 32 * w + k) * 1024 + lane * 16);
    SB;
    f32x4 a0, a1;
#pragma unroll
    for (int k = 0; k < 32; ++k) {
      if (k == 0) { mav0(a0, wo[k][0], of[k]); mav0(a1, wo[k][1], of[k]); } else { mav(a0, wo[k][0], of[k]); mav(a1, wo[k][1], of[k]); }
    }
    fin(a0, a1);
    char* red = lds + (s & 1) * 8192;
    *(f32x4*)(red + (w * 128 + lane) * 16) = a0; *(f32x4*)(red + (w * 128 + 64 + lane) * 16) = a1;
    lds_bar();
    STAMP(64 + s * 2 + 1);
    if (w == (s & 3)) {
      a0 = f32x4{0, 0, 0, 0}; a1 = a0;
#pragma unroll
      for (int i = 0; i < 4; ++i) { a0 += *(const f32x4*)(red + (i * 128 + lane) * 16); a1 += *(const f32x4*)(red + (i * 128 + 64 + lane) * 16); }
      u32x4 y;
#pragma unroll
      for (int r = 0; r < 4; ++r) y[r] = pk(bflo(xr[r]) + rbf(ga[2 * r] * rbf(a0[r])), bfhi(xr[r]) + rbf(ga[2 * r + 1] * rbf(a1[r])));
      st4(rw, OFF_X1 + row * 2, y);
      drain();
      if (lane == 0) addw(P.ws, OFF_X1C + (s * 2 + th) * 128);
    }
  }
}

template <int NCH, int LPR> DEV void ln_coef(u32x4 (&sh)[NCH], u32x4 (&sc)[NCH], const float* shift, const float* scl, int part) {
#pragma unroll
  for (int i = 0; i < NCH; ++i)
#pragma unroll
    for (int p = 0; p < 4; ++p) {
      const int d = (i * LPR + part) * 8 + 2 * p;
      const f32x2 a = *(const f32x2*)(shift + d), b = *(const f32x2*)(scl + d);
      sh[i][p] = pk(a[0], a[1]); sc[i][p] = pk(1.f + b[0], 1.f + b[1]);
    }
}
template <int NCH, int LPR> DEV void ln_mod(u32x4 (&v)[NCH], const u32x4 (&sh)[NCH], const u32x4 (&sc)[NCH]) {
  float sum = 0;
#pragma unroll
  for (int i = 0; i < NCH; ++i)
#pragma unroll
    for (int p = 0; p < 4; ++p) sum += bflo(v[i][p]) + bfhi(v[i][p]);
  const float mean = (LPR == 8 ? sum8(sum) : sum16(sum)) * (1.f / 512.f);
  float var = 0;
#pragma unroll
  for (int i = 0; i < NCH; ++i)
#pragma unroll
    for (int p = 0; p < 4; ++p) { float a = bflo(v[i][p]) - mean, b = bfhi(v[i][p]) - mean; var += a * a + b * b; }
  const float rstd = 1.f / sqrtf((LPR == 8 ? sum8(var) : sum16(var)) * (1.f / 512.f) + 1e-5f);
#pragma unroll
  for (int i = 0; i < NCH; ++i)
#pragma unroll
    for (int p = 0; p < 4; ++p)
      v[i][p] = pk(fmaf((bflo(v[i][p]) - mean) * rstd, bflo(sc[i][p]), bflo(sh[i][p])), fmaf((bfhi(v[i][p]) - mean) * rstd, bfhi(sc[i][p]), bfhi(sh[i][p])));
}

DEV void ln1_task(const Ptrs& P, rsrc_t rw, int s, int tid) {
  const float* mod = (const float*)(P.ws + OFF_MOD);
  const int tok = tid >> 3, part = tid & 7;
  u32x4 v[8];
#pragma unroll
  for (int i = 0; i < 8; ++i) v[i] = *(const u32x4*)(P.x + (s * 32 + tok) * 512 + (i * 8 + part) * 8);
  u32x4 sh[8], sc[8];
  ln_coef<8, 8>(sh, sc, mod, mod + 512, part);
  ln_mod<8, 8>(v, sh, sc);
#pragma unroll
  for (int i = 0; i < 8; ++i) st4(rw, OFF_H + s * 32768 + (((tok >> 4) * 16 + 2 * i + (part >> 2)) * 64 + (tok & 15) + 16 * (part & 3)) * 16, v[i]);
  drain();
  lds_bar();
  if (tid == 0) stw(P.ws, OFF_HF + s * 128, 1u);
  STAMP(3);
}

DEV void ffn1_role(const Ptrs& P, char* lds, rsrc_t rw, int fg, int th, int par, int tid, int lane, int w) {
  const int c = lane & 15, g = lane >> 4, kf = min(4 * fg + w, NKF - 1);
  const bool act = 4 * fg + w < NKF;
  const float* mod = (const float*)(P.ws + OFF_MOD);
  u32x4 w1[16][2];
  load_w2<16, false>(w1, P.w1, 32 * FF, FF, g * 8 * FF + 32 * kf + 2 * c);
  const u32x4 bb = *(const u32x4*)(P.b1 + 32 * kf + 8 * g);
  const int tok = tid >> 4, part = tid & 15;
  u32x4 sh[4], sc[4];
  ln_coef<4, 16>(sh, sc, mod + 1536, mod + 2048, part);
  for (int s = par; s < NSEG; s += 2) {
    wait_wg<12>(P.ws, OFF_X1C + (s * 2 + th) * 128, 16, w, s == NSEG - 1);
    STAMP(64 + s * 2);
    u32x4 v[4];
#pragma unroll
    for (int i = 0; i < 4; ++i) v[i] = ld4(rw, OFF_X1 + ((s * 32 + th * 16 + tok) * 512 + (i * 16 + part) * 8) * 2);
    ln_mod<4, 16>(v, sh, sc);
    char* lh = lds;
#pragma unroll
    for (int i = 0; i < 4; ++i) *(u32x4*)(lh + ((4 * i + (part >> 2)) * 64 + (tok ^ (part >> 2)) + 16 * (part & 3)) * 16) = v[i];
    lds_bar();
    f32x4 a0 = {}, a1 = {};
    u32x4 hf[16];
#pragma unroll
    for (int k = 0; k < 16; ++k) hf[k] = *(const u32x4*)(lh + (k * 64 + (lane ^ (k & 3))) * 16);
    SB;
#pragma unroll
    for (int k = 0; k < 16; ++k) { a0 = mma(w1[k][0], hf[k], a0); a1 = mma(w1[k][1], hf[k], a1); }
    u32x4 y;
#pragma unroll
    for (int r = 0; r < 4; ++r) {
      float u[2] = {rbf(rbf(a0[r]) + bflo(bb[r])), rbf(rbf(a1[r]) + bfhi(bb[r]))};
#pragma unroll
      for (int e = 0; e < 2; ++e) {
        float z = 1.5957691216057308f * (u[e] + 0.044715f * u[e] * u[e] * u[e]);
        u[e] = u[e] * __builtin_amdgcn_rcpf(1.f + __expf(-z));
      }
      y[r] = pk(u[0], u[1]);
    }
    if (act) st4(rw, OFF_G + ((s * 2 + th) * NKF + kf) * 1024 + lane * 16, y);
    drain();
    lds_bar();
    if (tid == 0) addw(P.ws, OFF_GC + (s * 2 + th) * 128);
    STAMP(64 + s * 2 + 1);
  }
}

DEV void ffn2_role(const Ptrs& P, char* lds, rsrc_t rw, int n, int th, int lane, int w) {
  const int c = lane & 15, g = lane >> 4;
  const float* mod = (const float*)(P.ws + OFF_MOD);
  u32x4 w2[11][4];
#pragma unroll
  for (int i = 0; i < 11; ++i) {
    const int kf = min(w + 4 * i, NKF - 1);
    u32x2 t[8];
#pragma unroll
    for (int j = 0; j < 8; ++j) t[j] = *(const u32x2*)(P.w2 + (size_t)(32 * kf + 8 * g + j) * 512 + 64 * n + 4 * c);
#pragma unroll
    for (int p = 0; p < 4; ++p) {
      w2[i][0][p] = ilo(t[2 * p][0], t[2 * p + 1][0]); w2[i][1][p] = ihi(t[2 * p][0], t[2 * p + 1][0]);
      w2[i][2][p] = ilo(t[2 * p][1], t[2 * p + 1][1]); w2[i][3][p] = ihi(t[2 * p][1], t[2 * p + 1][1]);
    }
  }
  const int col = 64 * n + 16 * g + 4 * w;
  const f32x4 gbv = *(const f32x4*)(mod + 2560 + col);
  const u32x2 b2 = *(const u32x2*)(P.b2 + col);
  for (int s = 0; s < NSEG; ++s) {
    wait_wg<12>(P.ws, OFF_GC + (s * 2 + th) * 128, 11, w, s == NSEG - 1);
    STAMP(64 + s * 2);
    u32x4 gf[11];
#pragma unroll
    for (int i = 0; i < 11; ++i) gf[i] = ld4(rw, OFF_G + ((s * 2 + th) * NKF + min(w + 4 * i, NKF - 1)) * 1024 + lane * 16);
    const u32 row = (s * 32 + th * 16 + c) * 512 + col;
    const u32x2 x1 = __builtin_amdgcn_raw_buffer_load_b64(rw, OFF_X1 + row * 2, 0, SC1);
    SB;
    if (w == 3) gf[10] = u32x4{0, 0, 0, 0};
    f32x4 acc[4] = {};
#pragma unroll
    for (int i = 0; i < 11; ++i)
#pragma unroll
      for (int t = 0; t < 4; ++t) acc[t] = mma(w2[i][t], gf[i], acc[t]);
    char* red = lds + (s & 1) * 16384;
#pragma unroll
    for (int r = 0; r < 4; ++r) *(f32x4*)(red + ((w * 4 + r) * 64 + lane) * 16) = f32x4{acc[0][r], acc[1][r], acc[2][r], acc[3][r]};
    lds_bar();
    f32x4 y = {};
#pragma unroll
    for (int i = 0; i < 4; ++i) y += *(const f32x4*)(red + ((i * 4 + w) * 64 + lane) * 16);
    u32x2 o;
#pragma unroll
    for (int p = 0; p < 2; ++p)
      o[p] = pk(bflo(x1[p]) + rbf(gbv[2 * p] * rbf(rbf(y[2 * p]) + bflo(b2[p]))), bfhi(x1[p]) + rbf(gbv[2 * p + 1] * rbf(rbf(y[2 * p + 1]) + bfhi(b2[p]))));
    *(u32x2*)(P.out + row) = o;
    STAMP(64 + s * 2 + 1);
  }
}

__global__ __launch_bounds__(256, 1) void main_kernel(const u16* x, const u16* mem0, const u16* wq, const u16* wk, const u16* wv, const u16* wo,
                                                      const u16* w1, const u16* b1, const u16* w2, const u16* b2, u16* out, char* ws) {
  __shared__ __attribute__((aligned(16))) char lds[LDS_BYTES];
  const Ptrs P = {x, mem0, wq, wk, wv, wo, w1, b1, w2, b2, out, ws};
  const int b = blockIdx.x, tid = threadIdx.x, lane = tid & 63, w = __builtin_amdgcn_readfirstlane(tid >> 6);
  const rsrc_t rw = __builtin_amdgcn_make_buffer_rsrc(ws, 0, WS_BYTES, 0x00020000);
  STAMP(0);
  if (b < 32) {
    chain_role(P, lds, rw, b, lane, w);
  } else if (b < 160) {
    proj_role(P, lds, rw, (b - 32) & 31, (b - 32) >> 5, tid, lane, w);
  } else if (b < 192) {
    for (int i = 0; i < 8; ++i) __builtin_amdgcn_s_sleep(36);
    oproj_role(P, lds, rw, (b - 160) >> 1, b & 1, lane, w);
  } else {
    ln1_task(P, rw, b - 192, tid);
    for (int i = 0; i < 8; ++i) __builtin_amdgcn_s_sleep(18);
    if (b < 208) ffn2_role(P, lds, rw, (b - 192) >> 1, b & 1, lane, w);
    else if (b < 252) ffn1_role(P, lds, rw, ((b - 208) % 22) >> 1, b & 1, (b - 208) / 22, tid, lane, w);
  }
}

extern "C" void kernel_launch(void* const* d_in, const int* in_sizes, int n_in, void* d_out, int out_size, void* d_ws, size_t ws_size,
                              hipStream_t stream) {
  const u16* const* in = (const u16* const*)d_in;
  setup_kernel<<<304, 256, 0, stream>>>(in[12], in[13], in[4], in[5], (char*)d_ws);
  main_kernel<<<256, 256, 0, stream>>>(in[0], in[1], in[2], in[3], in[4], in[5], in[8], in[9], in[10], in[11], (u16*)d_out, (char*)d_ws);
}
```

```cpp
#include <hip/hip_runtime.h>
#include <hip/hip_bf16.h>

typedef unsigned u32;
typedef unsigned long long u64;
typedef unsigned short u16;
typedef u32 u32x2 __attribute__((ext_vector_type(2)));
typedef u32 u32x4 __attribute__((ext_vector_type(4)));
typedef u32 u32x8 __attribute__((ext_vector_type(8)));
typedef float f32x4 __attribute__((ext_vector_type(4)));
typedef float f32x2 __attribute__((ext_vector_type(2)));
typedef short s16x4 __attribute__((ext_vector_type(4)));
typedef __bf16 bf16x2 __attribute__((ext_vector_type(2)));
typedef __bf16 bf16x8 __attribute__((ext_vector_type(8)));
typedef __bf16 bf16x16 __attribute__((ext_vector_type(16)));
typedef __amdgpu_buffer_rsrc_t rsrc_t;
#define DEV __device__ __forceinline__

constexpr int NSEG = 64, DM = 512, FF = 1376, NKF = 43;
constexpr u32 OFF_MACC = 0;
constexpr u32 OFF_KVF = 2u << 20;
constexpr u32 OFF_HF = OFF_KVF + 8192;
constexpr u32 OFF_RDY = OFF_HF + 8192;
constexpr u32 OFF_OC = OFF_RDY + 8192;
constexpr u32 OFF_X1C = OFF_OC + 8192;
constexpr u32 OFF_GC = OFF_X1C + 16384;
constexpr u32 ZERO_BYTES = OFF_GC + 16384;
constexpr u32 OFF_MOD = ZERO_BYTES;
constexpr u32 OFF_NRM = OFF_MOD + 12288;
constexpr u32 OFF_H = 3u << 20;
constexpr u32 OFF_KV = 5u << 20;
constexpr u32 OFF_O = 37u << 20;
constexpr u32 OFF_X1 = 53u << 20;
constexpr u32 OFF_G = 55u << 20;
constexpr u32 WS_BYTES = 61u << 20;
constexpr int LDS_BYTES = 163840;
constexpr int SPIN_CAP = 1 << 17;
constexpr int SC1 = 16;
constexpr float SM_C = 0.08838834764831845f * 1.4426950408889634f;

#define STAMP(id)
#define CYC(id)
struct Ptrs {
  const u16 *x, *mem0, *wq, *wk, *wv, *wo, *w1, *b1, *w2, *b2;
  u16* out;
  char* ws;
};

DEV float bflo(u32 v) { return __uint_as_float(v << 16); }
DEV float bfhi(u32 v) { return __uint_as_float(v & 0xffff0000u); }
DEV u32 pk(float a, float b) { bf16x2 v = {(__bf16)a, (__bf16)b}; return __builtin_bit_cast(u32, v); }
DEV float rbf(float a) { return (float)(__bf16)a; }
DEV u32 ilo(u32 a, u32 b) { return __builtin_amdgcn_perm(b, a, 0x05040100u); }
DEV u32 ihi(u32 a, u32 b) { return __builtin_amdgcn_perm(b, a, 0x07060302u); }
DEV f32x4 mma(u32x4 a, u32x4 b, f32x4 c) {
  return __builtin_amdgcn_mfma_f32_16x16x32_bf16(__builtin_bit_cast(bf16x8, a), __builtin_bit_cast(bf16x8, b), c, 0, 0, 0);
}
#define MMA_ASM(NAME, PRE, OP, TA, CA, CB) \
  DEV void NAME(f32x4& c, TA a, TA b) { asm(PRE OP " %0, %1, %2, %0" : "+v"(c) : CA(a), CB(b)); } \
  DEV void NAME##0(f32x4& c, TA a, TA b) { asm(PRE OP " %0, %1, %2, 0" : "=&v"(c) : CA(a), CB(b)); }
MMA_ASM(mvv, "s_nop 1\n\t", "v_mfma_f32_16x16x32_bf16", u32x4, "v", "v")
MMA_ASM(m16, "s_nop 1\n\t", "v_mfma_f32_16x16x16_bf16", u32x2, "v", "v")
MMA_ASM(mld, "", "v_mfma_f32_16x16x32_bf16", u32x4, "v", "v")
MMA_ASM(mav, "", "v_mfma_f32_16x16x32_bf16", u32x4, "a", "v")
MMA_ASM(mva, "", "v_mfma_f32_16x16x32_bf16", u32x4, "v", "a")
DEV void fin(f32x4& a) { asm volatile("s_nop 7" : "+v"(a)); }
DEV void fin(f32x4& a, f32x4& b) { asm volatile("s_nop 7" : "+v"(a), "+v"(b)); }
DEV void fin(f32x4& a, f32x4& b, f32x4& c) { asm volatile("s_nop 7" : "+v"(a), "+v"(b), "+v"(c)); }
DEV void fin(f32x4& a, f32x4& b, f32x4& c, f32x4& d) { asm volatile("s_nop 7" : "+v"(a), "+v"(b), "+v"(c), "+v"(d)); }
typedef __attribute__((address_space(3))) char* lptr;
DEV lptr lfix(char* p) { lptr q = (lptr)p; asm volatile("" : "+v"(q)); return q; }
#define LRD(p) (*(const __attribute__((address_space(3))) u32x4*)(p))
#define LWR(p) (*(__attribute__((address_space(3))) u32x4*)(p))
#define LW1(p) (*(__attribute__((address_space(3))) u32*)(p))
DEV f32x4 smma(u32x4 a, u32x8 b, f32x4 c, int idx) {
  return __builtin_amdgcn_smfmac_f32_16x16x64_bf16(__builtin_bit_cast(bf16x8, a), __builtin_bit_cast(bf16x16, b), c, idx, 0, 0);
}
DEV void smld(f32x4& c, u32x4 a, u32x8 b, int idx) {
  asm volatile("v_smfmac_f32_16x16x64_bf16 %0, %1, %2, %3" : "+v"(c) : "v"(a), "v"(b), "v"(idx));
}
DEV float swapadd(float a, float b) {
  auto t = __builtin_amdgcn_permlane32_swap(__float_as_uint(a), __float_as_uint(b), false, false);
  float r;
  asm("v_add_f32 %0, %1, %2" : "=v"(r) : "v"(t[0]), "v"(t[1]));
  return r;
}
DEV float fmax2(float a, float b) { float r; asm("v_max_f32 %0, %1, %2" : "=v"(r) : "v"(a), "v"(b)); return r; }
DEV float fmax3(float a, float b, float c) { float r; asm("v_max3_f32 %0, %1, %2, %3" : "=v"(r) : "v"(a), "v"(b), "v"(c)); return r; }
DEV float xmax(float x) {
  auto s = __builtin_amdgcn_permlane16_swap(__float_as_uint(x), __float_as_uint(x), false, false);
  x = fmax2(__uint_as_float(s[0]), __uint_as_float(s[1]));
  auto t = __builtin_amdgcn_permlane32_swap(__float_as_uint(x), __float_as_uint(x), false, false);
  return fmax2(__uint_as_float(t[0]), __uint_as_float(t[1]));
}
DEV int ixmax(int x) {
  auto s = __builtin_amdgcn_permlane16_swap((u32)x, (u32)x, false, false);
  x = max((int)s[0], (int)s[1]);
  auto t = __builtin_amdgcn_permlane32_swap((u32)x, (u32)x, false, false);
  return max((int)t[0], (int)t[1]);
}
template <int C> DEV int imaxd(int x) { return max(x, __builtin_amdgcn_update_dpp(x, x, C, 0xf, 0xf, true)); }
DEV int imax16(int x) { return imaxd<0x128>(imaxd<0x141>(imaxd<0x4E>(imaxd<0xB1>(x)))); }
DEV float xsum(float x) {
  auto s = __builtin_amdgcn_permlane16_swap(__float_as_uint(x), __float_as_uint(x), false, false);
  x = __uint_as_float(s[0]) + __uint_as_float(s[1]);
  auto t = __builtin_amdgcn_permlane32_swap(__float_as_uint(x), __float_as_uint(x), false, false);
  return __uint_as_float(t[0]) + __uint_as_float(t[1]);
}
template <int C> DEV float dpp(float x) { return __uint_as_float(__builtin_amdgcn_update_dpp(0, __float_as_uint(x), C, 0xf, 0xf, true)); }
DEV float sum8(float x) { x += dpp<0xB1>(x); x += dpp<0x4E>(x); return x + dpp<0x141>(x); }
DEV float sum16(float x) { x = sum8(x); return x + dpp<0x128>(x); }
DEV float max16(float x) { x = fmaxf(x, dpp<0xB1>(x)); x = fmaxf(x, dpp<0x4E>(x)); x = fmaxf(x, dpp<0x141>(x)); return fmaxf(x, dpp<0x128>(x)); }
DEV void lds_bar() { asm volatile("s_waitcnt lgkmcnt(0)\n\ts_barrier" ::: "memory"); }
#define SB __builtin_amdgcn_sched_barrier(0)
DEV void drain() { asm volatile("s_waitcnt vmcnt(0)" ::: "memory"); }
DEV u32x4 ld4(rsrc_t r, u32 off) { return __builtin_amdgcn_raw_buffer_load_b128(r, off, 0, SC1); }
DEV void st4(rsrc_t r, u32 off, u32x4 v) { __builtin_amdgcn_raw_buffer_store_b128(v, r, off, 0, SC1); }
DEV u32 ldw(const char* ws, u32 off) { return __hip_atomic_load((const u32*)(ws + off), __ATOMIC_RELAXED, __HIP_MEMORY_SCOPE_AGENT); }
DEV void stw(char* ws, u32 off, u32 v) { __hip_atomic_store((u32*)(ws + off), v, __ATOMIC_RELAXED, __HIP_MEMORY_SCOPE_AGENT); }
DEV void addw(char* ws, u32 off) { __hip_atomic_fetch_add((u32*)(ws + off), 1u, __ATOMIC_RELAXED, __HIP_MEMORY_SCOPE_AGENT); }
template <int SLEEP> DEV u32 wait_ge(const char* ws, u32 off, u32 target) {
  u32 v = 0;
#pragma unroll 1
  for (int n = 0; n < SPIN_CAP; ++n) {
    v = __builtin_amdgcn_readfirstlane(ldw(ws, off));
    if (v >= target) break;
    __builtin_amdgcn_s_sleep(SLEEP);
  }
  asm volatile("" ::: "memory");
  return v;
}
template <int SLEEP> DEV void wait_wg(const char* ws, u32 off, u32 target, int w, bool last = false) {
  if (w == 0) { if (last) wait_ge<1>(ws, off, target); else wait_ge<SLEEP>(ws, off, target); }
  lds_bar();
}
DEV f32x4 zv() { f32x4 z = {0, 0, 0, 0}; asm volatile("" : "+v"(z)); return z; }
DEV rsrc_t wrs(const u16* p) { return __builtin_amdgcn_make_buffer_rsrc((void*)p, 0, 0x7fffffff, 0x00020000); }
template <class T> DEV void pin_acc(T& v) { asm volatile("" : "+a"(v)); }
template <int NK, bool ACC> DEV void load_w2(u32x4 (&w)[NK][2], const u16* base, u32 kstep, u32 rstride, u32 loff) {
  constexpr int DEPTH = 7;
  u32 t[DEPTH + 1][8];
  const rsrc_t r = wrs(base);
#pragma unroll
  for (int ks = 0; ks < NK + DEPTH; ++ks) {
    if (ks < NK) {
#pragma unroll
      for (int j = 0; j < 8; ++j) t[ks % (DEPTH + 1)][j] = __builtin_amdgcn_raw_buffer_load_b32(r, loff * 2, (ks * kstep + j * rstride) * 2, 0);
    }
    SB;
    if (ks >= DEPTH) {
      const int k = ks - DEPTH;
#pragma unroll
      for (int p = 0; p < 4; ++p) {
        w[k][0][p] = ilo(t[k % (DEPTH + 1)][2 * p], t[k % (DEPTH + 1)][2 * p + 1]); w[k][1][p] = ihi(t[k % (DEPTH + 1)][2 * p], t[k % (DEPTH + 1)][2 * p + 1]);
      }
      if (ACC) { pin_acc(w[k][0]); pin_acc(w[k][1]); }
      SB;
    }
  }
}
DEV u32 dec(u32 lo, u32 hi, float inv) {
  const f32x2 v = __builtin_elementwise_fma(f32x2{(float)(int)lo, (float)(int)hi}, f32x2{inv, inv}, f32x2{976.f * inv, 0.f});
  return pk(v[0], v[1]);
}
DEV int poll_macc(rsrc_t r, u32 off, u32x4 (&q)[4]) {
  int n;
  for (n = 0; n < SPIN_CAP; ++n) {
    asm volatile("" ::: "memory");
#pragma unroll
    for (int i = 0; i < 4; ++i) q[i] = ld4(r, off + i * 2048);
    u32 m = 32;
#pragma unroll
    for (int i = 0; i < 4; ++i) m &= q[i][0] & q[i][2];
    if (__all(m != 0)) break;
  }
  return n;
}

__global__ __launch_bounds__(256) void setup_kernel(const u16* c, const u16* wmod, const u16* wq, const u16* wk, const u16* wv, const u16* wo, char* ws) {
  __shared__ float red[32 * 64];
  __shared__ float sc[512];
  const int b = blockIdx.x, tid = threadIdx.x;
  for (u32 i = b * 256 + tid; i < ZERO_BYTES / 16; i += gridDim.x * 256) ((u32x4*)ws)[i] = u32x4{0, 0, 0, 0};
  float* mod = (float*)(ws + OFF_MOD);
  float* nrm = (float*)(ws + OFF_NRM);
  if (b < 48) {
    u32x4 wr[16];
#pragma unroll
    for (int it = 0; it < 16; ++it) wr[it] = *(const u32x4*)(wmod + (size_t)(it * 32 + (tid >> 3)) * 3072 + b * 64 + (tid & 7) * 8);
    for (int i = tid; i < 512; i += 256) { float v = bflo(c[i]); sc[i] = rbf(v / (1.f + __expf(-v))); }
    __syncthreads();
    float acc[8] = {0, 0, 0, 0, 0, 0, 0, 0};
#pragma unroll
    for (int it = 0; it < 16; ++it) {
      float s = sc[it * 32 + (tid >> 3)];
#pragma unroll
      for (int p = 0; p < 4; ++p) { acc[2 * p] += s * bflo(wr[it][p]); acc[2 * p + 1] += s * bfhi(wr[it][p]); }
    }
#pragma unroll
    for (int j = 0; j < 8; ++j) red[(tid >> 3) * 64 + (tid & 7) * 8 + j] = acc[j];
    __syncthreads();
    if (tid < 64) { float s = 0; for (int i = 0; i < 32; ++i) s += red[i * 64 + tid]; mod[b * 64 + tid] = rbf(s); }
  } else {
    const int h = (b - 48) >> 3, part = (b - 48) & 7;
    u32x4 v[4], o[4], pq[4], pk4[4];
#pragma unroll
    for (int i = 0; i < 4; ++i) {
      v[i] = *(const u32x4*)(wv + (size_t)(part * 64 + i * 16 + (tid >> 4)) * 4096 + h * 128 + (tid & 15) * 8);
      o[i] = *(const u32x4*)(wo + (size_t)(h * 128 + i * 32 + (tid >> 3)) * 512 + part * 64 + (tid & 7) * 8);
      pq[i] = *(const u32x4*)(wq + (size_t)(part * 64 + i * 16 + (tid >> 4)) * 4096 + h * 128 + (tid & 15) * 8);
      pk4[i] = *(const u32x4*)(wk + (size_t)(part * 64 + i * 16 + (tid >> 4)) * 4096 + h * 128 + (tid & 15) * 8);
    }
    float a = 0, cs[8] = {0, 0, 0, 0, 0, 0, 0, 0};
#pragma unroll
    for (int i = 0; i < 4; ++i)
#pragma unroll
      for (int p = 0; p < 4; ++p) {
        a += bflo(v[i][p]) * bflo(v[i][p]) + bfhi(v[i][p]) * bfhi(v[i][p]);
        cs[2 * p] += bflo(o[i][p]) * bflo(o[i][p]); cs[2 * p + 1] += bfhi(o[i][p]) * bfhi(o[i][p]);
      }
#pragma unroll
    for (int j = 0; j < 8; ++j) red[(tid >> 3) * 64 + (tid & 7) * 8 + j] = cs[j];
    sc[tid] = a;
    __syncthreads();
    if (tid < 64) {
      float s = 0; for (int i = 0; i < 32; ++i) s += red[i * 64 + tid];
      float t = sc[tid] + sc[tid + 64] + sc[tid + 128] + sc[tid + 192];
      s = xmax(max16(s)); t = xsum(sum16(t));
      if (tid == 0) { nrm[h * 8 + part] = t; nrm[256 + h * 8 + part] = s; }
    }
#pragma unroll
    for (int i = 0; i < 4; ++i) asm volatile("" :: "v"(pq[i]), "v"(pk4[i]));
  }
}

DEV void chain_role(const Ptrs& P, char* lds, rsrc_t rw, int h, int lane, int w) {
  char* L_WV = lds + w * 32768;
  char* L_M = lds + 131072;
  char* L_Q = L_M + 8192;
  char* L_K = L_Q + 2304;
  char* L_O = L_K + 2304;
  const int c = lane & 15, g = lane >> 4;
  const int sidx = (lane & 8) ? 0xEEEE : 0x4444;
  const float* mod = (const float*)(P.ws + OFF_MOD);
  const float* nrm = (const float*)(P.ws + OFF_NRM);
  u32x8 wqk[4][8], wo[8][2];
  const int rot = (h + (h >> 3)) & 3;
  {
    const u32 loff = (32 * (g >> 1) + 2 * (g & 1)) * 4096;
    const rsrc_t vb = wrs(P.wv + h * 128 + (size_t)w * 128 * 4096);
    const rsrc_t wb = wrs((w < 2 ? P.wq : P.wk) + h * 128 + (w & 1) * 64);
    u32x4 tv[2][16];
    u32x2 tq[4][16];
#define WROW(s, p) (128 * ((s) >> 1) + 4 * ((s) & 1) + 64 * ((p) >> 3) + 16 * (((p) >> 1) & 1) + 8 * (((p) >> 2) & 1) + ((p) & 1))
#pragma unroll
    for (int i = 0; i < 32; ++i) tv[i >> 4][i & 15] = __builtin_amdgcn_raw_buffer_load_b128(vb, (loff + 8 * c) * 2, WROW(i >> 4, i & 15) * 8192, 0);
    SB;
#pragma unroll
    for (int s = 0; s < 11; ++s) {
      if (s < 8) {
#pragma unroll
        for (int p = 0; p < 16; ++p) tq[s & 3][p] = __builtin_amdgcn_raw_buffer_load_b64(wb, (loff + 4 * c) * 2, WROW(s, p) * 8192, 0);
      }
      SB;
      if (s < 2) {
        char* dst = lds + (c >> 2) * 32768 + ((2 * w + s) * 128 + 16 * g + 4 * (c & 3)) * 16;
#pragma unroll
        for (int u = 0; u < 4; ++u) {
          u32x8 f0, f1;
#pragma unroll
          for (int p = 0; p < 8; ++p) { f0[p] = ilo(tv[s][2 * p][u], tv[s][2 * p + 1][u]); f1[p] = ihi(tv[s][2 * p][u], tv[s][2 * p + 1][u]); }
          *(u32x4*)(dst + u * 16) = f0.lo; *(u32x4*)(dst + u * 16 + 1024) = f0.hi;
          *(u32x4*)(dst + u * 16 + 16384) = f1.lo; *(u32x4*)(dst + u * 16 + 17408) = f1.hi;
        }
      } else if (s >= 3) {
        const int k = s - 3;
#pragma unroll
        for (int p = 0; p < 8; ++p) {
          const u32x2 r0 = tq[k & 3][2 * p], r1 = tq[k & 3][2 * p + 1];
          wqk[0][k][p] = ilo(r0[0], r1[0]); wqk[1][k][p] = ihi(r0[0], r1[0]);
          wqk[2][k][p] = ilo(r0[1], r1[1]); wqk[3][k][p] = ihi(r0[1], r1[1]);
        }
#pragma unroll
        for (int t = 0; t < 4; ++t) pin_acc(wqk[t][k]);
      }
      SB;
    }
    const rsrc_t ob = wrs(P.wo + (size_t)h * 128 * 512 + 128 * w);
    const u32 ooff = (16 * (g >> 1) + 4 * (g & 1)) * 512 + 8 * c;
    u32x4 to[32];
#pragma unroll
    for (int i = 0; i < 32; ++i) to[i] = __builtin_amdgcn_raw_buffer_load_b128(ob, ooff * 2, (64 * (i >> 4) + 32 * ((i >> 3) & 1) + 8 * ((i >> 1) & 1) + 2 * ((i >> 2) & 1) + (i & 1)) * 1024, 0);
    SB;
#pragma unroll
    for (int i = 0; i < 16; ++i)
#pragma unroll
      for (int u = 0; u < 4; ++u) {
        const u32 r0 = to[2 * i][u], r1 = to[2 * i + 1][u];
        wo[2 * u][i >> 3][i & 7] = ilo(r0, r1); wo[2 * u + 1][i >> 3][i & 7] = ihi(r0, r1);
      }
  }
  const lptr kxr = lfix(lds + 147456 + lane * 16), kxw = lfix(lds + 147456 + w * 2048 + lane * 16);
  LWR(kxw + 8192) = wo[7][1].lo; LWR(kxw + 9216) = wo[7][1].hi;
  float gb, hx2;
  {
    float a = 0, b = 0;
#pragma unroll
    for (int i = 0; i < 8; ++i) { a += nrm[(lane & 31) * 8 + i]; b = fmaxf(b, nrm[256 + (lane & 31) * 8 + i]); }
    gb = 0.5f * 1.03f * xsum(sum16(sqrtf(a * b)));
    float s2 = 0, cm = 0;
#pragma unroll
    for (int i = 0; i < 8; ++i) { float sa = mod[lane * 8 + i]; s2 += sa * sa; cm = fmaxf(cm, fabsf(1.f + mod[512 + lane * 8 + i])); }
    float hx = 1.01f * (22.6275f * xmax(max16(cm)) + sqrtf(xsum(sum16(s2))));
    hx2 = hx * hx;
  }
  const int mtok = lane >> 3, msub = lane & 7;
  const lptr mw = lfix(L_M + (2 * w * 64 + msub * 8 + (mtok ^ msub)) * 16);
  const lptr mr = lfix(L_M + ((lane >> 3) * 8 + ((lane & 7) ^ (lane >> 3))) * 16);
  {
    const u16* a = P.mem0 + mtok * 512 + 128 * w + 16 * msub;
#pragma unroll
    for (int f = 0; f < 2; ++f) {
      u32x2 x0 = *(const u32x2*)(a + 4 * f), x1 = *(const u32x2*)(a + 4 * f + 8);
      LWR(mw + f * 1024) = u32x4{x0[0], x1[0], x0[1], x1[1]};
    }
  }
  const u32 kvf = OFF_KVF + h * 256;
  u32x4 ks[2], vx[2];
  STAMP(1);
  wait_ge<2>(P.ws, kvf, 1);
  STAMP(2);
#pragma unroll
  for (int a = 0; a < 2; ++a) { ks[a] = ld4(rw, OFF_KV + h * 16384 + (2 * w + a) * 1024 + lane * 16); vx[a] = ld4(rw, OFF_KV + h * 16384 + 8192 + (2 * w + a) * 1024 + lane * 16); }
  LWR(kxw) = ks[0]; LWR(kxw + 1024) = ks[1];
  u32 fl = ldw(P.ws, kvf + 4), kb0 = OFF_KV + h * 16384 + 2 * w * 1024 + lane * 16, fs = __builtin_amdgcn_readfirstlane(fl);
  const lptr qkw = lfix((w < 2 ? L_Q : L_K) + (8 * (w & 1) + (c >> 1)) * 144 + 64 * (g & 1) + 4 * (c & 1) + 8 * (lane >> 5));
  const lptr qkr = lfix(L_Q + g * 144 + (lane & 7) * 16);
  const lptr wvr = lfix(L_WV + lane * 16);
  const lptr ow = lfix(L_O + ((4 * w + g) * 8 + (lane & 7)) * 16), orr = lfix(L_O + (lane ^ rot) * 16);
  u32 aoff[4];
#pragma unroll
  for (int r = 0; r < 4; ++r) { aoff[r] = (w * 32 + (lane >> 5) * 8 + 4 * (g & 1) + (r ^ rot)) * 256 + c * 8; asm volatile("" : "+v"(aoff[r])); }
  const u32 poff = (w * 32 + mtok) * 256 + msub * 16;
  lds_bar();

  f32x4 aq[4] = {zv(), zv(), zv(), zv()}, av[2] = {zv(), zv()};
  const u32x2 onem = {g < 2 ? 0x3f803f80u : 0u, g < 2 ? 0x3f803f80u : 0u};
  for (int s = 0; s < NSEG - 1; ++s) {
    STAMP(64 + s); CYC(5);
    f32x4 gm, sm, s0, s1;
    u32x4 mf[8];
    u32x8 wb[2][2];
#define LDV(k) _Pragma("unroll") for (int a = 0; a < 2; ++a) { wb[(k) & 1][a].lo = LRD(wvr + (a * 16 + (k) * 2) * 1024); wb[(k) & 1][a].hi = LRD(wvr + (a * 16 + (k) * 2 + 1) * 1024); }
#define MMV(k) { smld(av[0], mf[k], wb[(k) & 1][0], sidx); smld(av[1], mf[k], wb[(k) & 1][1], sidx); }
#define MQK(k) _Pragma("unroll") for (int t = 0; t < 4; ++t) aq[t] = smma(mf[k], wqk[t][k], aq[t], sidx);
#pragma unroll
    for (int k = 0; k < 8; ++k) mf[k] = LRD(mr + k * 1024);
    LDV(0);
    SB;
    if (fs == 0) wait_ge<2>(P.ws, kvf + 4 * (s + 1), 1);
    fl = ldw(P.ws, kvf + 4 * (s + 2));
    SB;
    MQK(0); SB; LDV(1); SB;
    MQK(1); mld0(gm, mf[0], mf[0]); mld(gm, mf[1], mf[1]); SB;
    MQK(2); mld(gm, mf[2], mf[2]); mld(gm, mf[3], mf[3]); mld(gm, mf[4], mf[4]); SB;
    MMV(0); SB; LDV(2); mld(gm, mf[5], mf[5]); mld(gm, mf[6], mf[6]); mld(gm, mf[7], mf[7]); SB;
    MQK(3); SB;
    MMV(1); SB; LDV(3); SB;
    asm volatile("" : "+v"(gm));
    int n2 = max(max(__float_as_int(gm[0]), __float_as_int(gm[1])), max(__float_as_int(gm[2]), __float_as_int(gm[3])));
#define MQ1(k, t) SB; aq[t] = smma(mf[k], wqk[t][k], aq[t], sidx); SB;
    MQ1(4, 0); n2 = imaxd<0xB1>(n2); MQ1(4, 1); n2 = imaxd<0x4E>(n2); MQ1(4, 2); n2 = imaxd<0x141>(n2); MQ1(4, 3); n2 = imaxd<0x128>(n2);
    MQ1(5, 0); auto p16 = __builtin_amdgcn_permlane16_swap((u32)n2, (u32)n2, false, false);
    MQ1(5, 1); n2 = max((int)p16[0], (int)p16[1]);
    MQ1(5, 2); auto p32 = __builtin_amdgcn_permlane32_swap((u32)n2, (u32)n2, false, false);
    MQ1(5, 3); float inv = fmaxf(2.f * __int_as_float(max((int)p32[0], (int)p32[1])), hx2);
    MQ1(6, 0); inv = __builtin_amdgcn_sqrtf(inv);
    MQ1(6, 1); inv = fmaxf(inv * gb, 1e-20f) * (1.f / 2080374784.f);
    MQ1(6, 2); const u32 kb = kb0 + ((s + 1) & 15) * (32 * 16384);
    MQ1(6, 3);
    MQK(7); SB;
    MMV(2); SB;
    LDV(4);
    const u32 e0 = pk(swapadd(aq[0][0], aq[1][0]), swapadd(aq[2][0], aq[3][0])), e1 = pk(swapadd(aq[0][1], aq[1][1]), swapadd(aq[2][1], aq[3][1]));
    const u32 e2 = pk(swapadd(aq[0][2], aq[1][2]), swapadd(aq[2][2], aq[3][2])), e3 = pk(swapadd(aq[0][3], aq[1][3]), swapadd(aq[2][3], aq[3][3]));
    LW1(qkw) = e0; LW1(qkw + 16) = e1; LW1(qkw + 32) = e2; LW1(qkw + 48) = e3;
    SB;
    MMV(3); SB;
    u32x4 qf[4], kf[4], kx[8];
    LDV(5); kx[0] = LRD(kxr); kx[4] = LRD(kxr + 4096);
    SB;
    asm volatile("s_waitcnt lgkmcnt(6)\n\ts_barrier" ::: "memory");
    CYC(0);
    qf[0] = LRD(qkr); kf[0] = LRD(qkr + 2304); qf[1] = LRD(qkr + 576); kf[1] = LRD(qkr + 2304 + 576);
    SB;
    MMV(4); SB;
    kx[1] = LRD(kxr + 1024); kx[5] = LRD(kxr + 5120); qf[2] = LRD(qkr + 1152); kf[2] = LRD(qkr + 2304 + 1152);
    SB;
    MMV(5); SB;
    kx[2] = LRD(kxr + 2048); kx[6] = LRD(kxr + 6144); qf[3] = LRD(qkr + 1728); kf[3] = LRD(qkr + 2304 + 1728);
    SB;
    mld0(s0, kx[0], qf[0]); mld0(s1, kx[4], qf[0]); mld0(sm, kf[0], qf[0]);
    SB; kx[3] = LRD(kxr + 3072); kx[7] = LRD(kxr + 7168); SB;
    mld(s0, kx[1], qf[1]); mld(s1, kx[5], qf[1]); mld(sm, kf[1], qf[1]);
    mld(s0, kx[2], qf[2]); mld(s1, kx[6], qf[2]); mld(sm, kf[2], qf[2]);
    SB; LDV(6); SB;
    mld(s0, kx[3], qf[3]); mld(s1, kx[7], qf[3]); mld(sm, kf[3], qf[3]);
    SB; LDV(7); SB;
    u32x2 o1, o2;
    asm volatile("v_mov_b64 %0, %5\n\tv_mov_b64 %1, %5\n\ts_nop 1" : "=v"(o1), "=v"(o2), "+v"(s0), "+v"(s1), "+v"(sm) : "s"(0x3f803f803f803f80ull));
    const u32x4 one = {o1[0], o1[1], o2[0], o2[1]};
    float mx = fmax2(fmax3(fmax3(s0[0], s0[1], s0[2]), fmax3(s0[3], s1[0], s1[1]), fmax3(s1[2], s1[3], sm[0])), fmax3(sm[1], sm[2], sm[3]));
    mx = xmax(mx) * SM_C;
    SB; MMV(6); SB;
#pragma unroll
    for (int r = 0; r < 4; ++r) s0[r] = __builtin_amdgcn_exp2f(fmaf(s0[r], SM_C, -mx));
    SB; MMV(7); SB;
#pragma unroll
    for (int r = 0; r < 4; ++r) s1[r] = __builtin_amdgcn_exp2f(fmaf(s1[r], SM_C, -mx));
    u32x4 px = {pk(s0[0], s0[1]), pk(s0[2], s0[3]), pk(s1[0], s1[1]), pk(s1[2], s1[3])};
    f32x4 o[2], rsum;
    SB;
    mvv0(rsum, one, px); mld0(o[0], vx[0], px); mld0(o[1], vx[1], px);
    SB;
#pragma unroll
    for (int a = 0; a < 2; ++a) { ks[a] = ld4(rw, kb + a * 1024); vx[a] = __builtin_amdgcn_raw_buffer_load_b128(rw, kb + a * 1024, 8192, SC1); }
    SB;
    const u32x2 vm0 = {pk(av[0][0], av[0][1]), pk(av[0][2], av[0][3])}, vm1 = {pk(av[1][0], av[1][1]), pk(av[1][2], av[1][3])};
#pragma unroll
    for (int r = 0; r < 4; ++r) sm[r] = __builtin_amdgcn_exp2f(fmaf(sm[r], SM_C, -mx));
    u32x2 pm = {pk(sm[0], sm[1]), pk(sm[2], sm[3])};
    SB;
    m16(rsum, onem, pm);
    asm("v_mfma_f32_16x16x16_bf16 %0, %2, %4, %0\n\tv_mfma_f32_16x16x16_bf16 %1, %3, %4, %1" : "+v"(o[0]), "+v"(o[1]) : "v"(vm0), "v"(vm1), "v"(pm));
    SB;
    asm volatile("s_nop 4" : "+v"(rsum));
    float rs = __builtin_amdgcn_rcpf(rsum[0] * inv);
    asm volatile("s_nop 0" : "+v"(o[0]), "+v"(o[1]), "+v"(rs));
    if (!(lane & 8))
      LWR(ow) = u32x4{pk(o[0][0] * rs, o[1][0] * rs), pk(o[0][1] * rs, o[1][1] * rs), pk(o[0][2] * rs, o[1][2] * rs), pk(o[0][3] * rs, o[1][3] * rs)};
    lds_bar();
    CYC(1);
    u32x4 of0 = LRD(orr), of1 = LRD(orr + 1024);
    wo[7][1].lo = LRD(kxw + 8192); wo[7][1].hi = LRD(kxw + 9216);
    SB;
    f32x4 t[8] = {zv(), zv(), zv(), zv(), zv(), zv(), zv(), zv()};
    char* ab[2] = {P.ws + OFF_MACC + (s + 1) * 32768, P.ws + OFF_MACC + (s + 1) * 32768 + 4096};
    SB;
#define ENC(q, r) { u32 lo = ((u32)(int)swapadd(t[4 * q][r], t[4 * q + 2][r]) & ~63u) | 1u, hi = (u32)(int)swapadd(t[4 * q + 1][r], t[4 * q + 3][r]); \
      asm volatile("global_atomic_add_x2 %0, %1, %2" :: "v"(aoff[r]), "v"(u32x2{lo, hi}), "s"(ab[q]) : "memory"); }
#pragma unroll
    for (int i = 0; i < 4; ++i) smld(t[i], of0, wo[i][0], sidx);
#pragma unroll
    for (int i = 0; i < 4; ++i) smld(t[i], of1, wo[i][1], sidx);
    SB;
#pragma unroll
    for (int r = 0; r < 4; ++r) {
      smld(t[4 + (2 * r & 3)], r < 2 ? of0 : of1, wo[4 + (2 * r & 3)][r >> 1], sidx);
      smld(t[5 + (2 * r & 3)], r < 2 ? of0 : of1, wo[5 + (2 * r & 3)][r >> 1], sidx);
      if (r == 0) asm volatile("s_nop 5" : "+v"(t[0]), "+v"(t[1]), "+v"(t[2]), "+v"(t[3]));
      SB;
      ENC(0, r);
      SB;
    }
    asm volatile("" : "+v"(t[4]), "+v"(t[5]), "+v"(t[6]), "+v"(t[7]));
#pragma unroll
    for (int r = 0; r < 4; ++r) ENC(1, r);
    CYC(2);
#pragma unroll
    for (int i = 0; i < 4; ++i) aq[i] = zv();
    av[0] = zv(); av[1] = zv();
    drain();
    CYC(3);
    fs = __builtin_amdgcn_readfirstlane(fl);
    LWR(kxw) = ks[0]; LWR(kxw + 1024) = ks[1];
    int rounds;
#pragma unroll 1
    for (rounds = 0; rounds < SPIN_CAP; ++rounds) {
      asm volatile("" ::: "memory");
      u32x4 q[4];
#pragma unroll
      for (int i = 0; i < 4; ++i) q[i] = ld4(rw, OFF_MACC + (s + 1) * 32768 + poff + i * 2048);
      SB;
      u32 m = 32;
#pragma unroll
      for (int f = 0; f < 2; ++f) {
        const u32 d0 = dec(q[2 * f][0], q[2 * f][1], inv), d1 = dec(q[2 * f][2], q[2 * f][3], inv);
        SB;
        m &= q[2 * f][0] & q[2 * f][2] & q[2 * f + 1][0] & q[2 * f + 1][2];
        LWR(mw + f * 1024) = u32x4{d0, d1, dec(q[2 * f + 1][0], q[2 * f + 1][1], inv), dec(q[2 * f + 1][2], q[2 * f + 1][3], inv)};
        SB;
      }
      if (__all(m != 0)) break;
    }
    CYC(4);
    lds_bar();
    if (h == 0 && w == 0 && lane == 0) stw(P.ws, OFF_RDY + (s + 1) * 128, __float_as_uint(inv));
  }
}

DEV void proj_role(const Ptrs& P, char* lds, rsrc_t rw, int h, int r0, int tid, int lane, int w) {
  char* L_H = lds;
  char* L_S = lds + 32768;
  char* L_MT = lds + 81920;
  char* L_KM = lds + 98304;
  const int c = lane & 15, g = lane >> 4;
  u32x4 wq[16][2], wk[16][2], wv[16][2];
  const u32 loff = g * 8 * 4096 + h * 128 + 32 * w + 2 * c;
  load_w2<16, true>(wk, P.wk, 32 * 4096, 4096, loff);
  load_w2<16, true>(wv, P.wv, 32 * 4096, 4096, loff);
#pragma unroll
  for (int i = 0; i < 4; ++i) *(u32x4*)(L_MT + (i * 256 + tid) * 16) = u32x4{0, 0, 0, 0};
  auto kv_part = [&](int s, char* slot) __attribute__((always_inline)) {
    wait_wg<8>(P.ws, OFF_HF + s * 128, 1, w);
    u32x4 t[8];
#pragma unroll
    for (int j = 0; j < 8; ++j) t[j] = ld4(rw, OFF_H + s * 32768 + (j * 256 + tid) * 16);
#pragma unroll
    for (int j = 0; j < 8; ++j) *(u32x4*)(L_H + (j * 256 + tid) * 16) = t[j];
    lds_bar();
    const u32 kb = OFF_KV + ((s & 15) * 32 + h) * 16384 + lane * 16;
    u32x2 vp[2][2];
#pragma unroll
    for (int mt = 0; mt < 2; ++mt) {
      f32x4 ak[2], av[2];
      u32x4 hfa[16];
#pragma unroll
      for (int ks = 0; ks < 16; ++ks) hfa[ks] = *(const u32x4*)(L_H + ((mt * 16 + ks) * 64 + lane) * 16);
      SB;
#pragma unroll
      for (int ks = 0; ks < 16; ++ks) {
        const u32x4 hf = hfa[ks];
#pragma unroll
        for (int a = 0; a < 2; ++a) {
          if (ks == 0) { mav0(ak[a], wk[ks][a], hf); mva0(av[a], hf, wv[ks][a]); } else { mav(ak[a], wk[ks][a], hf); mva(av[a], hf, wv[ks][a]); }
        }
      }
      fin(ak[0], ak[1], av[0], av[1]);
      u32x4 kf = {pk(ak[0][0], ak[0][1]), pk(ak[0][2], ak[0][3]), pk(ak[1][0], ak[1][1]), pk(ak[1][2], ak[1][3])};
      st4(rw, kb + (mt * 4 + w) * 1024, kf);
      *(u32x4*)(slot + 8192 + ((mt * 4 + w) * 64 + lane) * 16) = kf;
#pragma unroll
      for (int a = 0; a < 2; ++a) vp[mt][a] = u32x2{pk(av[a][0], av[a][1]), pk(av[a][2], av[a][3])};
    }
#pragma unroll
    for (int a = 0; a < 2; ++a) {
      u32x4 vf = {vp[0][a][0], vp[0][a][1], vp[1][a][0], vp[1][a][1]};
      st4(rw, kb + 8192 + (2 * w + a) * 1024, vf);
      *(u32x4*)(slot + 16384 + ((2 * w + a) * 64 + lane) * 16) = vf;
    }
  };
  kv_part(r0, L_S);
  drain();
  lds_bar();
  if (tid == 0) stw(P.ws, OFF_KVF + h * 256 + r0 * 4, 1u);
  load_w2<16, false>(wq, P.wq, 32 * 4096, 4096, loff);
  for (int i = 0; i <= 16; ++i) {
    if (i < 16) {
      const int s = 4 * i + r0;
      char* slot = L_S + (i & 1) * 24576;
      STAMP(64 + i * 4);
      if (i > 0) kv_part(s, slot);
#pragma unroll
      for (int mt = 0; mt < 2; ++mt) {
        f32x4 aq[2];
#pragma unroll
        for (int ks = 0; ks < 16; ++ks) {
          u32x4 hf = *(const u32x4*)(L_H + ((mt * 16 + ks) * 64 + lane) * 16);
#pragma unroll
          for (int a = 0; a < 2; ++a) { if (ks == 0) mld0(aq[a], wq[ks][a], hf); else mld(aq[a], wq[ks][a], hf); }
        }
        fin(aq[0], aq[1]);
        *(u32x4*)(slot + ((mt * 4 + w) * 64 + lane) * 16) = u32x4{pk(aq[0][0], aq[0][1]), pk(aq[0][2], aq[0][3]), pk(aq[1][0], aq[1][1]), pk(aq[1][2], aq[1][3])};
      }
      drain();
      lds_bar();
      if (tid == 0) stw(P.ws, OFF_KVF + h * 256 + s * 4, 1u);
      STAMP(64 + i * 4 + 1);
    }
    if (i >= 1) {
      const int s = 4 * (i - 1) + r0;
      const char* slot = L_S + ((i - 1) & 1) * 24576 + lane * 16;
      f32x4 x00, x01, x11;
#pragma unroll
      for (int k = 0; k < 4; ++k) {
        u32x4 q0 = *(const u32x4*)(slot + k * 1024), q1 = *(const u32x4*)(slot + (4 + k) * 1024);
        u32x4 k0 = *(const u32x4*)(slot + (8 + k) * 1024), k1 = *(const u32x4*)(slot + (12 + k) * 1024);
        if (k == 0) { mld0(x00, k0, q0); mld0(x01, k0, q1); mld0(x11, k1, q1); } else { mld(x00, k0, q0); mld(x01, k0, q1); mld(x11, k1, q1); }
      }
      fin(x00, x01, x11);
      const int tok = lane >> 3, sub = lane & 7;
      if (s == 0) {
#pragma unroll
        for (int j = 0; j < 2; ++j) {
          const int u = j * 256 + tid, t = u >> 6, cg = u & 63;
          *(u32x4*)(L_MT + ((cg >> 2) * 64 + t + 16 * (cg & 3)) * 16) = *(const u32x4*)(P.mem0 + t * 512 + cg * 8);
        }
      } else {
        wait_wg<12>(P.ws, OFF_RDY + s * 128, 1, w, s == NSEG - 1);
        const float inv = __uint_as_float(ldw(P.ws, OFF_RDY + s * 128));
        STAMP(64 + (i - 1) * 4 + 2);
        u32x4 q[4];
        poll_macc(rw, OFF_MACC + s * 32768 + (w * 32 + tok) * 256 + sub * 16, q);
#pragma unroll
        for (int u = 0; u < 4; ++u)
#pragma unroll
          for (int e = 0; e < 2; ++e)
            *(u32*)(L_MT + ((4 * w + (sub >> 1)) * 64 + tok + 16 * ((2 * sub + e) & 3)) * 16 + 4 * u) = dec(q[u][2 * e], q[u][2 * e + 1], inv);
      }
      lds_bar();
      f32x4 ak[2], av[2];
      u32x4 mfa[16];
#pragma unroll
      for (int ks = 0; ks < 16; ++ks) mfa[ks] = *(const u32x4*)(L_MT + (ks * 64 + lane) * 16);
      SB;
#pragma unroll
      for (int ks = 0; ks < 16; ++ks) {
        const u32x4 mf = mfa[ks];
#pragma unroll
        for (int a = 0; a < 2; ++a) {
          if (ks == 0) { mav0(ak[a], wk[ks][a], mf); mva0(av[a], mf, wv[ks][a]); } else { mav(ak[a], wk[ks][a], mf); mva(av[a], mf, wv[ks][a]); }
        }
      }
      fin(ak[0], ak[1], av[0], av[1]);
      *(u32x4*)(L_KM + (w * 64 + lane) * 16) = u32x4{pk(ak[0][0], ak[0][1]), pk(ak[0][2], ak[0][3]), pk(ak[1][0], ak[1][1]), pk(ak[1][2], ak[1][3])};
      lds_bar();
      f32x4 m0, m1;
#pragma unroll
      for (int k = 0; k < 4; ++k) {
        u32x4 km = *(const u32x4*)(L_KM + (k * 64 + lane) * 16);
        u32x4 q0 = *(const u32x4*)(slot + k * 1024), q1 = *(const u32x4*)(slot + (4 + k) * 1024);
        if (k == 0) { mld0(m0, km, q0); mld0(m1, km, q1); } else { mld(m0, km, q0); mld(m1, km, q1); }
      }
      fin(m0, m1);
      const float NEG = -__builtin_inff();
#pragma unroll
      for (int r = 0; r < 4; ++r) {
        if (g >= 2) { m0[r] = NEG; m1[r] = NEG; }
        if (4 * g + r > c) { x00[r] = NEG; x11[r] = NEG; }
      }
      float a0 = xmax(fmaxf(fmaxf(fmaxf(m0[0], m0[1]), fmaxf(m0[2], m0[3])), fmaxf(fmaxf(x00[0], x00[1]), fmaxf(x00[2], x00[3])))) * SM_C;
      float a1 = xmax(fmaxf(fmaxf(fmaxf(fmaxf(m1[0], m1[1]), fmaxf(m1[2], m1[3])), fmaxf(fmaxf(x01[0], x01[1]), fmaxf(x01[2], x01[3]))),
                            fmaxf(fmaxf(x11[0], x11[1]), fmaxf(x11[2], x11[3])))) * SM_C;
      float t0 = 0, t1 = 0;
#pragma unroll
      for (int r = 0; r < 4; ++r) {
        m0[r] = __builtin_amdgcn_exp2f(fmaf(m0[r], SM_C, -a0)); x00[r] = __builtin_amdgcn_exp2f(fmaf(x00[r], SM_C, -a0));
        m1[r] = __builtin_amdgcn_exp2f(fmaf(m1[r], SM_C, -a1)); x01[r] = __builtin_amdgcn_exp2f(fmaf(x01[r], SM_C, -a1)); x11[r] = __builtin_amdgcn_exp2f(fmaf(x11[r], SM_C, -a1));
        t0 += m0[r] + x00[r]; t1 += m1[r] + x01[r] + x11[r];
      }
      t0 = __builtin_amdgcn_rcpf(xsum(t0)); t1 = __builtin_amdgcn_rcpf(xsum(t1));
      u32x2 pm0 = {pk(m0[0] * t0, m0[1] * t0), pk(m0[2] * t0, m0[3] * t0)}, pm1 = {pk(m1[0] * t1, m1[1] * t1), pk(m1[2] * t1, m1[3] * t1)};
      u32x4 px0 = {pk(x00[0] * t0, x00[1] * t0), pk(x00[2] * t0, x00[3] * t0), 0u, 0u};
      u32x4 px1 = {pk(x01[0] * t1, x01[1] * t1), pk(x01[2] * t1, x01[3] * t1), pk(x11[0] * t1, x11[1] * t1), pk(x11[2] * t1, x11[3] * t1)};
      f32x4 o0[2], o1[2];
#pragma unroll
      for (int a = 0; a < 2; ++a) {
        u32x4 vf = *(const u32x4*)(slot + (16 + 2 * w + a) * 1024);
        mvv0(o0[a], vf, px0); mvv0(o1[a], vf, px1);
      }
      fin(o0[0], o0[1], o1[0], o1[1]);
#pragma unroll
      for (int a = 0; a < 2; ++a) {
        u32x2 vm = {pk(av[a][0], av[a][1]), pk(av[a][2], av[a][3])};
        m16(o0[a], vm, pm0); m16(o1[a], vm, pm1);
      }
      fin(o0[0], o0[1], o1[0], o1[1]);
      const u32 ob = OFF_O + (s * 256 + 4 * h + w) * 1024 + lane * 16;
      st4(rw, ob, u32x4{pk(o0[0][0], o0[1][0]), pk(o0[0][1], o0[1][1]), pk(o0[0][2], o0[1][2]), pk(o0[0][3], o0[1][3])});
      st4(rw, ob + 131072, u32x4{pk(o1[0][0], o1[1][0]), pk(o1[0][1], o1[1][1]), pk(o1[0][2], o1[1][2]), pk(o1[0][3], o1[1][3])});
      drain();
      lds_bar();
      if (tid == 0) addw(P.ws, OFF_OC + s * 128);
      STAMP(64 + (i - 1) * 4 + 3);
    }
  }
}

DEV void oproj_role(const Ptrs& P, char* lds, rsrc_t rw, int n, int th, int lane, int w) {
  const int c = lane & 15, g = lane >> 4;
  const float* mod = (const float*)(P.ws + OFF_MOD);
  u32x4 wo[32][2];
  load_w2<32, true>(wo, P.wo, 32 * 512, 512, (w * 1024 + g * 8) * 512 + 32 * n + 2 * c);
  float ga[8];
#pragma unroll
  for (int j = 0; j < 8; ++j) ga[j] = mod[1024 + 32 * n + 8 * g + j];
  for (int s = 0; s < NSEG; ++s) {
    const u32 row = (s * 32 + th * 16 + c) * 512 + 32 * n + 8 * g;
    u32x4 xr = *(const u32x4*)(P.x + row);
    wait_wg<12>(P.ws, OFF_OC + s * 128, 32, w, s == NSEG - 1);
    STAMP(64 + s * 2);
    u32x4 of[32];
#pragma unroll
    for (int k = 0; k < 32; ++k) of[k] = ld4(rw, OFF_O + ((s * 2 + th) * 128 + 32 * w + k) * 1024 + lane * 16);
    SB;
    f32x4 a0, a1;
#pragma unroll
    for (int k = 0; k < 32; ++k) {
      if (k == 0) { mav0(a0, wo[k][0], of[k]); mav0(a1, wo[k][1], of[k]); } else { mav(a0, wo[k][0], of[k]); mav(a1, wo[k][1], of[k]); }
    }
    fin(a0, a1);
    char* red = lds + (s & 1) * 8192;
    *(f32x4*)(red + (w * 128 + lane) * 16) = a0; *(f32x4*)(red + (w * 128 + 64 + lane) * 16) = a1;
    lds_bar();
    STAMP(64 + s * 2 + 1);
    if (w == (s & 3)) {
      a0 = f32x4{0, 0, 0, 0}; a1 = a0;
#pragma unroll
      for (int i = 0; i < 4; ++i) { a0 += *(const f32x4*)(red + (i * 128 + lane) * 16); a1 += *(const f32x4*)(red + (i * 128 + 64 + lane) * 16); }
      u32x4 y;
#pragma unroll
      for (int r = 0; r < 4; ++r) y[r] = pk(bflo(xr[r]) + rbf(ga[2 * r] * rbf(a0[r])), bfhi(xr[r]) + rbf(ga[2 * r + 1] * rbf(a1[r])));
      st4(rw, OFF_X1 + row * 2, y);
      drain();
      if (lane == 0) addw(P.ws, OFF_X1C + (s * 2 + th) * 128);
    }
  }
}

template <int NCH, int LPR> DEV void ln_coef(u32x4 (&sh)[NCH], u32x4 (&sc)[NCH], const float* shift, const float* scl, int part) {
#pragma unroll
  for (int i = 0; i < NCH; ++i)
#pragma unroll
    for (int p = 0; p < 4; ++p) {
      const int d = (i * LPR + part) * 8 + 2 * p;
      const f32x2 a = *(const f32x2*)(shift + d), b = *(const f32x2*)(scl + d);
      sh[i][p] = pk(a[0], a[1]); sc[i][p] = pk(1.f + b[0], 1.f + b[1]);
    }
}
template <int NCH, int LPR> DEV void ln_mod(u32x4 (&v)[NCH], const u32x4 (&sh)[NCH], const u32x4 (&sc)[NCH]) {
  float sum = 0;
#pragma unroll
  for (int i = 0; i < NCH; ++i)
#pragma unroll
    for (int p = 0; p < 4; ++p) sum += bflo(v[i][p]) + bfhi(v[i][p]);
  const float mean = (LPR == 8 ? sum8(sum) : sum16(sum)) * (1.f / 512.f);
  float var = 0;
#pragma unroll
  for (int i = 0; i < NCH; ++i)
#pragma unroll
    for (int p = 0; p < 4; ++p) { float a = bflo(v[i][p]) - mean, b = bfhi(v[i][p]) - mean; var += a * a + b * b; }
  const float rstd = 1.f / sqrtf((LPR == 8 ? sum8(var) : sum16(var)) * (1.f / 512.f) + 1e-5f);
#pragma unroll
  for (int i = 0; i < NCH; ++i)
#pragma unroll
    for (int p = 0; p < 4; ++p)
      v[i][p] = pk(fmaf((bflo(v[i][p]) - mean) * rstd, bflo(sc[i][p]), bflo(sh[i][p])), fmaf((bfhi(v[i][p]) - mean) * rstd, bfhi(sc[i][p]), bfhi(sh[i][p])));
}

DEV void ln1_task(const Ptrs& P, rsrc_t rw, int s, int tid) {
  const float* mod = (const float*)(P.ws + OFF_MOD);
  const int tok = tid >> 3, part = tid & 7;
  u32x4 v[8];
#pragma unroll
  for (int i = 0; i < 8; ++i) v[i] = *(const u32x4*)(P.x + (s * 32 + tok) * 512 + (i * 8 + part) * 8);
  u32x4 sh[8], sc[8];
  ln_coef<8, 8>(sh, sc, mod, mod + 512, part);
  ln_mod<8, 8>(v, sh, sc);
#pragma unroll
  for (int i = 0; i < 8; ++i) st4(rw, OFF_H + s * 32768 + (((tok >> 4) * 16 + 2 * i + (part >> 2)) * 64 + (tok & 15) + 16 * (part & 3)) * 16, v[i]);
  drain();
  lds_bar();
  if (tid == 0) stw(P.ws, OFF_HF + s * 128, 1u);
  STAMP(3);
}

DEV void ffn1_role(const Ptrs& P, char* lds, rsrc_t rw, int fg, int th, int par, int tid, int lane, int w) {
  const int c = lane & 15, g = lane >> 4, kf = min(4 * fg + w, NKF - 1);
  const bool act = 4 * fg + w < NKF;
  const float* mod = (const float*)(P.ws + OFF_MOD);
  u32x4 w1[16][2];
  load_w2<16, false>(w1, P.w1, 32 * FF, FF, g * 8 * FF + 32 * kf + 2 * c);
  const u32x4 bb = *(const u32x4*)(P.b1 + 32 * kf + 8 * g);
  const int tok = tid >> 4, part = tid & 15;
  u32x4 sh[4], sc[4];
  ln_coef<4, 16>(sh, sc, mod + 1536, mod + 2048, part);
  for (int s = par; s < NSEG; s += 2) {
    wait_wg<12>(P.ws, OFF_X1C + (s * 2 + th) * 128, 16, w, s == NSEG - 1);
    STAMP(64 + s * 2);
    u32x4 v[4];
#pragma unroll
    for (int i = 0; i < 4; ++i) v[i] = ld4(rw, OFF_X1 + ((s * 32 + th * 16 + tok) * 512 + (i * 16 + part) * 8) * 2);
    ln_mod<4, 16>(v, sh, sc);
    char* lh = lds;
#pragma unroll
    for (int i = 0; i < 4; ++i) *(u32x4*)(lh + ((4 * i + (part >> 2)) * 64 + (tok ^ (part >> 2)) + 16 * (part & 3)) * 16) = v[i];
    lds_bar();
    f32x4 a0 = {}, a1 = {};
    u32x4 hf[16];
#pragma unroll
    for (int k = 0; k < 16; ++k) hf[k] = *(const u32x4*)(lh + (k * 64 + (lane ^ (k & 3))) * 16);
    SB;
#pragma unroll
    for (int k = 0; k < 16; ++k) { a0 = mma(w1[k][0], hf[k], a0); a1 = mma(w1[k][1], hf[k], a1); }
    u32x4 y;
#pragma unroll
    for (int r = 0; r < 4; ++r) {
      float u[2] = {rbf(rbf(a0[r]) + bflo(bb[r])), rbf(rbf(a1[r]) + bfhi(bb[r]))};
#pragma unroll
      for (int e = 0; e < 2; ++e) {
        float z = 1.5957691216057308f * (u[e] + 0.044715f * u[e] * u[e] * u[e]);
        u[e] = u[e] * __builtin_amdgcn_rcpf(1.f + __expf(-z));
      }
      y[r] = pk(u[0], u[1]);
    }
    if (act) st4(rw, OFF_G + ((s * 2 + th) * NKF + kf) * 1024 + lane * 16, y);
    drain();
    lds_bar();
    if (tid == 0) addw(P.ws, OFF_GC + (s * 2 + th) * 128);
    STAMP(64 + s * 2 + 1);
  }
}

DEV void ffn2_role(const Ptrs& P, char* lds, rsrc_t rw, int n, int th, int lane, int w) {
  const int c = lane & 15, g = lane >> 4;
  const float* mod = (const float*)(P.ws + OFF_MOD);
  u32x4 w2[11][4];
#pragma unroll
  for (int i = 0; i < 11; ++i) {
    const int kf = min(w + 4 * i, NKF - 1);
    u32x2 t[8];
#pragma unroll
    for (int j = 0; j < 8; ++j) t[j] = *(const u32x2*)(P.w2 + (size_t)(32 * kf + 8 * g + j) * 512 + 64 * n + 4 * c);
#pragma unroll
    for (int p = 0; p < 4; ++p) {
      w2[i][0][p] = ilo(t[2 * p][0], t[2 * p + 1][0]); w2[i][1][p] = ihi(t[2 * p][0], t[2 * p + 1][0]);
      w2[i][2][p] = ilo(t[2 * p][1], t[2 * p + 1][1]); w2[i][3][p] = ihi(t[2 * p][1], t[2 * p + 1][1]);
    }
  }
  const int col = 64 * n + 16 * g + 4 * w;
  const f32x4 gbv = *(const f32x4*)(mod + 2560 + col);
  const u32x2 b2 = *(const u32x2*)(P.b2 + col);
  for (int s = 0; s < NSEG; ++s) {
    wait_wg<12>(P.ws, OFF_GC + (s * 2 + th) * 128, 11, w, s == NSEG - 1);
    STAMP(64 + s * 2);
    u32x4 gf[11];
#pragma unroll
    for (int i = 0; i < 11; ++i) gf[i] = ld4(rw, OFF_G + ((s * 2 + th) * NKF + min(w + 4 * i, NKF - 1)) * 1024 + lane * 16);
    const u32 row = (s * 32 + th * 16 + c) * 512 + col;
    const u32x2 x1 = __builtin_amdgcn_raw_buffer_load_b64(rw, OFF_X1 + row * 2, 0, SC1);
    SB;
    if (w == 3) gf[10] = u32x4{0, 0, 0, 0};
    f32x4 acc[4] = {};
#pragma unroll
    for (int i = 0; i < 11; ++i)
#pragma unroll
      for (int t = 0; t < 4; ++t) acc[t] = mma(w2[i][t], gf[i], acc[t]);
    char* red = lds + (s & 1) * 16384;
#pragma unroll
    for (int r = 0; r < 4; ++r) *(f32x4*)(red + ((w * 4 + r) * 64 + lane) * 16) = f32x4{acc[0][r], acc[1][r], acc[2][r], acc[3][r]};
    lds_bar();
    f32x4 y = {};
#pragma unroll
    for (int i = 0; i < 4; ++i) y += *(const f32x4*)(red + ((i * 4 + w) * 64 + lane) * 16);
    u32x2 o;
#pragma unroll
    for (int p = 0; p < 2; ++p)
      o[p] = pk(bflo(x1[p]) + rbf(gbv[2 * p] * rbf(rbf(y[2 * p]) + bflo(b2[p]))), bfhi(x1[p]) + rbf(gbv[2 * p + 1] * rbf(rbf(y[2 * p + 1]) + bfhi(b2[p]))));
    *(u32x2*)(P.out + row) = o;
    STAMP(64 + s * 2 + 1);
  }
}

__global__ __launch_bounds__(256, 1) void main_kernel(const u16* x, const u16* mem0, const u16* wq, const u16* wk, const u16* wv, const u16* wo,
                                                      const u16* w1, const u16* b1, const u16* w2, const u16* b2, u16* out, char* ws) {
  __shared__ __attribute__((aligned(16))) char lds[LDS_BYTES];
  const Ptrs P = {x, mem0, wq, wk, wv, wo, w1, b1, w2, b2, out, ws};
  const int b = blockIdx.x, tid = threadIdx.x, lane = tid & 63, w = __builtin_amdgcn_readfirstlane(tid >> 6);
  const rsrc_t rw = __builtin_amdgcn_make_buffer_rsrc(ws, 0, WS_BYTES, 0x00020000);
  STAMP(0);
  if (b < 32) {
    chain_role(P, lds, rw, b, lane, w);
  } else if (b < 160) {
    proj_role(P, lds, rw, (b - 32) & 31, (b - 32) >> 5, tid, lane, w);
  } else if (b < 192) {
    for (int i = 0; i < 8; ++i) __builtin_amdgcn_s_sleep(36);
    oproj_role(P, lds, rw, (b - 160) >> 1, b & 1, lane, w);
  } else {
    ln1_task(P, rw, b - 192, tid);
    for (int i = 0; i < 8; ++i) __builtin_amdgcn_s_sleep(18);
    if (b < 208) ffn2_role(P, lds, rw, (b - 192) >> 1, b & 1, lane, w);
    else if (b < 252) ffn1_role(P, lds, rw, ((b - 208) % 22) >> 1, b & 1, (b - 208) / 22, tid, lane, w);
  }
}

extern "C" void kernel_launch(void* const* d_in, const int* in_sizes, int n_in, void* d_out, int out_size, void* d_ws, size_t ws_size,
                              hipStream_t stream) {
  const u16* const* in = (const u16* const*)d_in;
  setup_kernel<<<304, 256, 0, stream>>>(in[12], in[13], in[2], in[3], in[4], in[5], (char*)d_ws);
  main_kernel<<<256, 256, 0, stream>>>(in[0], in[1], in[2], in[3], in[4], in[5], in[8], in[9], in[10], in[11], (u16*)d_out, (char*)d_ws);
}
```

```cpp
#include <hip/hip_runtime.h>
#include <hip/hip_bf16.h>

typedef unsigned u32;
typedef unsigned long long u64;
typedef unsigned short u16;
typedef u32 u32x2 __attribute__((ext_vector_type(2)));
typedef u32 u32x4 __attribute__((ext_vector_type(4)));
typedef u32 u32x8 __attribute__((ext_vector_type(8)));
typedef float f32x4 __attribute__((ext_vector_type(4)));
typedef float f32x2 __attribute__((ext_vector_type(2)));
typedef short s16x4 __attribute__((ext_vector_type(4)));
typedef __bf16 bf16x2 __attribute__((ext_vector_type(2)));
typedef __bf16 bf16x8 __attribute__((ext_vector_type(8)));
typedef __bf16 bf16x16 __attribute__((ext_vector_type(16)));
typedef __amdgpu_buffer_rsrc_t rsrc_t;
#define DEV __device__ __forceinline__

constexpr int NSEG = 64, DM = 512, FF = 1376, NKF = 43;
constexpr u32 OFF_MACC = 0;
constexpr u32 OFF_KVF = 2u << 20;
constexpr u32 OFF_HF = OFF_KVF + 8192;
constexpr u32 OFF_RDY = OFF_HF + 8192;
constexpr u32 OFF_OC = OFF_RDY + 8192;
constexpr u32 OFF_X1C = OFF_OC + 8192;
constexpr u32 OFF_GC = OFF_X1C + 16384;
constexpr u32 ZERO_BYTES = OFF_GC + 16384;
constexpr u32 OFF_MOD = ZERO_BYTES;
constexpr u32 OFF_NRM = OFF_MOD + 12288;
constexpr u32 OFF_H = 3u << 20;
constexpr u32 OFF_KV = 5u << 20;
constexpr u32 OFF_O = 37u << 20;
constexpr u32 OFF_X1 = 53u << 20;
constexpr u32 OFF_G = 55u << 20;
constexpr u32 WS_BYTES = 61u << 20;
constexpr int LDS_BYTES = 163840;
constexpr int SPIN_CAP = 1 << 17;
constexpr int SC1 = 16;
constexpr int BG = 64, BGX = 80, MID = 16, TAILN = 6;
constexpr float SM_C = 0.08838834764831845f * 1.4426950408889634f;

struct Ptrs {
  const u16 *x, *mem0, *wq, *wk, *wv, *wo, *w1, *b1, *w2, *b2;
  u16* out;
  char* ws;
};

DEV float bflo(u32 v) { return __uint_as_float(v << 16); }
DEV float bfhi(u32 v) { return __uint_as_float(v & 0xffff0000u); }
DEV u32 pk(float a, float b) { bf16x2 v = {(__bf16)a, (__bf16)b}; return __builtin_bit_cast(u32, v); }
DEV float rbf(float a) { return (float)(__bf16)a; }
DEV u32 ilo(u32 a, u32 b) { return __builtin_amdgcn_perm(b, a, 0x05040100u); }
DEV u32 ihi(u32 a, u32 b) { return __builtin_amdgcn_perm(b, a, 0x07060302u); }
DEV f32x4 mma(u32x4 a, u32x4 b, f32x4 c) {
  return __builtin_amdgcn_mfma_f32_16x16x32_bf16(__builtin_bit_cast(bf16x8, a), __builtin_bit_cast(bf16x8, b), c, 0, 0, 0);
}
#define MMA_ASM(NAME, PRE, OP, TA, CA, CB) \
  DEV void NAME(f32x4& c, TA a, TA b) { asm(PRE OP " %0, %1, %2, %0" : "+v"(c) : CA(a), CB(b)); } \
  DEV void NAME##0(f32x4& c, TA a, TA b) { asm(PRE OP " %0, %1, %2, 0" : "=&v"(c) : CA(a), CB(b)); }
MMA_ASM(mvv, "s_nop 1\n\t", "v_mfma_f32_16x16x32_bf16", u32x4, "v", "v")
MMA_ASM(m16, "s_nop 1\n\t", "v_mfma_f32_16x16x16_bf16", u32x2, "v", "v")
MMA_ASM(mld, "", "v_mfma_f32_16x16x32_bf16", u32x4, "v", "v")
MMA_ASM(mav, "", "v_mfma_f32_16x16x32_bf16", u32x4, "a", "v")
MMA_ASM(mva, "", "v_mfma_f32_16x16x32_bf16", u32x4, "v", "a")
DEV void fin(f32x4& a) { asm volatile("s_nop 7" : "+v"(a)); }
DEV void fin(f32x4& a, f32x4& b) { asm volatile("s_nop 7" : "+v"(a), "+v"(b)); }
DEV void fin(f32x4& a, f32x4& b, f32x4& c) { asm volatile("s_nop 7" : "+v"(a), "+v"(b), "+v"(c)); }
DEV void fin(f32x4& a, f32x4& b, f32x4& c, f32x4& d) { asm volatile("s_nop 7" : "+v"(a), "+v"(b), "+v"(c), "+v"(d)); }
typedef __attribute__((address_space(3))) char* lptr;
DEV lptr lfix(char* p) { lptr q = (lptr)p; asm volatile("" : "+v"(q)); return q; }
#define LRD(p) (*(const __attribute__((address_space(3))) u32x4*)(p))
#define LWR(p) (*(__attribute__((address_space(3))) u32x4*)(p))
#define LW1(p) (*(__attribute__((address_space(3))) u32*)(p))
DEV f32x4 smma(u32x4 a, u32x8 b, f32x4 c, int idx) {
  return __builtin_amdgcn_smfmac_f32_16x16x64_bf16(__builtin_bit_cast(bf16x8, a), __builtin_bit_cast(bf16x16, b), c, idx, 0, 0);
}
DEV void smld(f32x4& c, u32x4 a, u32x8 b, int idx) {
  asm volatile("v_smfmac_f32_16x16x64_bf16 %0, %1, %2, %3" : "+v"(c) : "v"(a), "v"(b), "v"(idx));
}
DEV float swapadd(float a, float b) {
  auto t = __builtin_amdgcn_permlane32_swap(__float_as_uint(a), __float_as_uint(b), false, false);
  float r;
  asm("v_add_f32 %0, %1, %2" : "=v"(r) : "v"(t[0]), "v"(t[1]));
  return r;
}
DEV float fmax2(float a, float b) { float r; asm("v_max_f32 %0, %1, %2" : "=v"(r) : "v"(a), "v"(b)); return r; }
DEV float fmax3(float a, float b, float c) { float r; asm("v_max3_f32 %0, %1, %2, %3" : "=v"(r) : "v"(a), "v"(b), "v"(c)); return r; }
DEV float xmax(float x) {
  auto s = __builtin_amdgcn_permlane16_swap(__float_as_uint(x), __float_as_uint(x), false, false);
  x = fmax2(__uint_as_float(s[0]), __uint_as_float(s[1]));
  auto t = __builtin_amdgcn_permlane32_swap(__float_as_uint(x), __float_as_uint(x), false, false);
  return fmax2(__uint_as_float(t[0]), __uint_as_float(t[1]));
}
template <int C> DEV int imaxd(int x) { return max(x, __builtin_amdgcn_update_dpp(x, x, C, 0xf, 0xf, true)); }
DEV float xsum(float x) {
  auto s = __builtin_amdgcn_permlane16_swap(__float_as_uint(x), __float_as_uint(x), false, false);
  x = __uint_as_float(s[0]) + __uint_as_float(s[1]);
  auto t = __builtin_amdgcn_permlane32_swap(__float_as_uint(x), __float_as_uint(x), false, false);
  return __uint_as_float(t[0]) + __uint_as_float(t[1]);
}
template <int C> DEV float dpp(float x) { return __uint_as_float(__builtin_amdgcn_update_dpp(0, __float_as_uint(x), C, 0xf, 0xf, true)); }
DEV float sum8(float x) { x += dpp<0xB1>(x); x += dpp<0x4E>(x); return x + dpp<0x141>(x); }
DEV float sum16(float x) { x = sum8(x); return x + dpp<0x128>(x); }
DEV float max16(float x) { x = fmaxf(x, dpp<0xB1>(x)); x = fmaxf(x, dpp<0x4E>(x)); x = fmaxf(x, dpp<0x141>(x)); return fmaxf(x, dpp<0x128>(x)); }
DEV void lds_bar() { asm volatile("s_waitcnt lgkmcnt(0)\n\ts_barrier" ::: "memory"); }
#define SB __builtin_amdgcn_sched_barrier(0)
DEV void drain() { asm volatile("s_waitcnt vmcnt(0)" ::: "memory"); }
DEV u32x4 ld4(rsrc_t r, u32 off) { return __builtin_amdgcn_raw_buffer_load_b128(r, off, 0, SC1); }
DEV void st4(rsrc_t r, u32 off, u32x4 v) { __builtin_amdgcn_raw_buffer_store_b128(v, r, off, 0, SC1); }
DEV u32 ldw(const char* ws, u32 off) { return __hip_atomic_load((const u32*)(ws + off), __ATOMIC_RELAXED, __HIP_MEMORY_SCOPE_AGENT); }
DEV void stw(char* ws, u32 off, u32 v) { __hip_atomic_store((u32*)(ws + off), v, __ATOMIC_RELAXED, __HIP_MEMORY_SCOPE_AGENT); }
DEV void addw(char* ws, u32 off) { __hip_atomic_fetch_add((u32*)(ws + off), 1u, __ATOMIC_RELAXED, __HIP_MEMORY_SCOPE_AGENT); }
template <int SLEEP> DEV u32 wait_ge(const char* ws, u32 off, u32 target) {
  u32 v = 0;
#pragma unroll 1
  for (int n = 0; n < SPIN_CAP; ++n) {
    v = __builtin_amdgcn_readfirstlane(ldw(ws, off));
    if (v >= target) break;
    __builtin_amdgcn_s_sleep(SLEEP);
  }
  asm volatile("" ::: "memory");
  return v;
}
template <int SLEEP> DEV void wait_wg(const char* ws, u32 off, u32 target, int w, int s = 0) {
  if (w == 0) {
    if (s == NSEG - 1) wait_ge<1>(ws, off, target);
    else if (s >= NSEG - TAILN) wait_ge<MID>(ws, off, target);
    else wait_ge<SLEEP>(ws, off, target);
  }
  lds_bar();
}
DEV f32x4 zv() { f32x4 z = {0, 0, 0, 0}; asm volatile("" : "+v"(z)); return z; }
DEV rsrc_t wrs(const u16* p) { return __builtin_amdgcn_make_buffer_rsrc((void*)p, 0, 0x7fffffff, 0x00020000); }
template <class T> DEV void pin_acc(T& v) { asm volatile("" : "+a"(v)); }
template <int NK, bool ACC> DEV void load_w2(u32x4 (&w)[NK][2], const u16* base, u32 kstep, u32 rstride, u32 loff) {
  constexpr int DEPTH = 7;
  u32 t[DEPTH + 1][8];
  const rsrc_t r = wrs(base);
#pragma unroll
  for (int ks = 0; ks < NK + DEPTH; ++ks) {
    if (ks < NK) {
#pragma unroll
      for (int j = 0; j < 8; ++j) t[ks % (DEPTH + 1)][j] = __builtin_amdgcn_raw_buffer_load_b32(r, loff * 2, (ks * kstep + j * rstride) * 2, 0);
    }
    SB;
    if (ks >= DEPTH) {
      const int k = ks - DEPTH;
#pragma unroll
      for (int p = 0; p < 4; ++p) {
        w[k][0][p] = ilo(t[k % (DEPTH + 1)][2 * p], t[k % (DEPTH + 1)][2 * p + 1]); w[k][1][p] = ihi(t[k % (DEPTH + 1)][2 * p], t[k % (DEPTH + 1)][2 * p + 1]);
      }
      if (ACC) { pin_acc(w[k][0]); pin_acc(w[k][1]); }
      SB;
    }
  }
}
DEV u32 dec(u32 lo, u32 hi, float inv) {
  const f32x2 v = __builtin_elementwise_fma(f32x2{(float)(int)lo, (float)(int)hi}, f32x2{inv, inv}, f32x2{976.f * inv, 0.f});
  return pk(v[0], v[1]);
}
DEV void poll_macc(rsrc_t r, u32 off, u32x4 (&q)[4]) {
  for (int n = 0; n < SPIN_CAP; ++n) {
    asm volatile("" ::: "memory");
#pragma unroll
    for (int i = 0; i < 4; ++i) q[i] = ld4(r, off + i * 2048);
    u32 m = 32;
#pragma unroll
    for (int i = 0; i < 4; ++i) m &= q[i][0] & q[i][2];
    if (__all(m != 0)) break;
  }
}

__global__ __launch_bounds__(256) void setup_kernel(const u16* c, const u16* wmod, const u16* wq, const u16* wk, const u16* wv, const u16* wo, char* ws) {
  __shared__ float red[32 * 64];
  __shared__ float sc[512];
  const int b = blockIdx.x, tid = threadIdx.x;
  for (u32 i = b * 256 + tid; i < ZERO_BYTES / 16; i += gridDim.x * 256) ((u32x4*)ws)[i] = u32x4{0, 0, 0, 0};
  float* mod = (float*)(ws + OFF_MOD);
  float* nrm = (float*)(ws + OFF_NRM);
  if (b < 48) {
    u32x4 wr[16];
#pragma unroll
    for (int it = 0; it < 16; ++it) wr[it] = *(const u32x4*)(wmod + (size_t)(it * 32 + (tid >> 3)) * 3072 + b * 64 + (tid & 7) * 8);
    for (int i = tid; i < 512; i += 256) { float v = bflo(c[i]); sc[i] = rbf(v / (1.f + __expf(-v))); }
    __syncthreads();
    float acc[8] = {0, 0, 0, 0, 0, 0, 0, 0};
#pragma unroll
    for (int it = 0; it < 16; ++it) {
      float s = sc[it * 32 + (tid >> 3)];
#pragma unroll
      for (int p = 0; p < 4; ++p) { acc[2 * p] += s * bflo(wr[it][p]); acc[2 * p + 1] += s * bfhi(wr[it][p]); }
    }
#pragma unroll
    for (int j = 0; j < 8; ++j) red[(tid >> 3) * 64 + (tid & 7) * 8 + j] = acc[j];
    __syncthreads();
    if (tid < 64) { float s = 0; for (int i = 0; i < 32; ++i) s += red[i * 64 + tid]; mod[b * 64 + tid] = rbf(s); }
  } else {
    const int h = (b - 48) >> 3, part = (b - 48) & 7;
    u32x4 v[4], o[4], pq[4], pk4[4];
#pragma unroll
    for (int i = 0; i < 4; ++i) {
      v[i] = *(const u32x4*)(wv + (size_t)(part * 64 + i * 16 + (tid >> 4)) * 4096 + h * 128 + (tid & 15) * 8);
      o[i] = *(const u32x4*)(wo + (size_t)(h * 128 + i * 32 + (tid >> 3)) * 512 + part * 64 + (tid & 7) * 8);
      pq[i] = *(const u32x4*)(wq + (size_t)(part * 64 + i * 16 + (tid >> 4)) * 4096 + h * 128 + (tid & 15) * 8);
      pk4[i] = *(const u32x4*)(wk + (size_t)(part * 64 + i * 16 + (tid >> 4)) * 4096 + h * 128 + (tid & 15) * 8);
    }
    float a = 0, cs[8] = {0, 0, 0, 0, 0, 0, 0, 0};
#pragma unroll
    for (int i = 0; i < 4; ++i)
#pragma unroll
      for (int p = 0; p < 4; ++p) {
        a += bflo(v[i][p]) * bflo(v[i][p]) + bfhi(v[i][p]) * bfhi(v[i][p]);
        cs[2 * p] += bflo(o[i][p]) * bflo(o[i][p]); cs[2 * p + 1] += bfhi(o[i][p]) * bfhi(o[i][p]);
      }
#pragma unroll
    for (int j = 0; j < 8; ++j) red[(tid >> 3) * 64 + (tid & 7) * 8 + j] = cs[j];
    sc[tid] = a;
    __syncthreads();
    if (tid < 64) {
      float s = 0; for (int i = 0; i < 32; ++i) s += red[i * 64 + tid];
      float t = sc[tid] + sc[tid + 64] + sc[tid + 128] + sc[tid + 192];
      s = xmax(max16(s)); t = xsum(sum16(t));
      if (tid == 0) { nrm[h * 8 + part] = t; nrm[256 + h * 8 + part] = s; }
    }
#pragma unroll
    for (int i = 0; i < 4; ++i) asm volatile("" :: "v"(pq[i]), "v"(pk4[i]));
  }
}

DEV void chain_role(const Ptrs& P, char* lds, rsrc_t rw, int h, int lane, int w) {
  char* L_WV = lds + w * 32768;
  char* L_M = lds + 131072;
  char* L_Q = L_M + 8192;
  char* L_K = L_Q + 2304;
  char* L_O = L_K + 2304;
  const int c = lane & 15, g = lane >> 4;
  const int sidx = (lane & 8) ? 0xEEEE : 0x4444;
  const float* mod = (const float*)(P.ws + OFF_MOD);
  const float* nrm = (const float*)(P.ws + OFF_NRM);
  u32x8 wqk[4][8], wo[8][2];
  const int rot = (h + (h >> 3)) & 3;
  {
    const u32 loff = (32 * (g >> 1) + 2 * (g & 1)) * 4096;
    const rsrc_t vb = wrs(P.wv + h * 128 + (size_t)w * 128 * 4096);
    const rsrc_t wb = wrs((w < 2 ? P.wq : P.wk) + h * 128 + (w & 1) * 64);
    u32x4 tv[2][16];
    u32x2 tq[4][16];
#define WROW(s, p) (128 * ((s) >> 1) + 4 * ((s) & 1) + 64 * ((p) >> 3) + 16 * (((p) >> 1) & 1) + 8 * (((p) >> 2) & 1) + ((p) & 1))
#pragma unroll
    for (int i = 0; i < 32; ++i) tv[i >> 4][i & 15] = __builtin_amdgcn_raw_buffer_load_b128(vb, (loff + 8 * c) * 2, WROW(i >> 4, i & 15) * 8192, 0);
    SB;
#pragma unroll
    for (int s = 0; s < 11; ++s) {
      if (s < 8) {
#pragma unroll
        for (int p = 0; p < 16; ++p) tq[s & 3][p] = __builtin_amdgcn_raw_buffer_load_b64(wb, (loff + 4 * c) * 2, WROW(s, p) * 8192, 0);
      }
      SB;
      if (s < 2) {
        char* dst = lds + (c >> 2) * 32768 + ((2 * w + s) * 128 + 16 * g + 4 * (c & 3)) * 16;
#pragma unroll
        for (int u = 0; u < 4; ++u) {
          u32x8 f0, f1;
#pragma unroll
          for (int p = 0; p < 8; ++p) { f0[p] = ilo(tv[s][2 * p][u], tv[s][2 * p + 1][u]); f1[p] = ihi(tv[s][2 * p][u], tv[s][2 * p + 1][u]); }
          *(u32x4*)(dst + u * 16) = f0.lo; *(u32x4*)(dst + u * 16 + 1024) = f0.hi;
          *(u32x4*)(dst + u * 16 + 16384) = f1.lo; *(u32x4*)(dst + u * 16 + 17408) = f1.hi;
        }
      } else if (s >= 3) {
        const int k = s - 3;
#pragma unroll
        for (int p = 0; p < 8; ++p) {
          const u32x2 r0 = tq[k & 3][2 * p], r1 = tq[k & 3][2 * p + 1];
          wqk[0][k][p] = ilo(r0[0], r1[0]); wqk[1][k][p] = ihi(r0[0], r1[0]);
          wqk[2][k][p] = ilo(r0[1], r1[1]); wqk[3][k][p] = ihi(r0[1], r1[1]);
        }
#pragma unroll
        for (int t = 0; t < 4; ++t) pin_acc(wqk[t][k]);
      }
      SB;
    }
    const rsrc_t ob = wrs(P.wo + (size_t)h * 128 * 512 + 128 * w);
    const u32 ooff = (16 * (g >> 1) + 4 * (g & 1)) * 512 + 8 * c;
    u32x4 to[32];
#pragma unroll
    for (int i = 0; i < 32; ++i) to[i] = __builtin_amdgcn_raw_buffer_load_b128(ob, ooff * 2, (64 * (i >> 4) + 32 * ((i >> 3) & 1) + 8 * ((i >> 1) & 1) + 2 * ((i >> 2) & 1) + (i & 1)) * 1024, 0);
    SB;
#pragma unroll
    for (int i = 0; i < 16; ++i)
#pragma unroll
      for (int u = 0; u < 4; ++u) {
        const u32 r0 = to[2 * i][u], r1 = to[2 * i + 1][u];
        wo[2 * u][i >> 3][i & 7] = ilo(r0, r1); wo[2 * u + 1][i >> 3][i & 7] = ihi(r0, r1);
      }
  }
  const lptr kxr = lfix(lds + 147456 + lane * 16), kxw = lfix(lds + 147456 + w * 2048 + lane * 16);
  LWR(kxw + 8192) = wo[7][1].lo; LWR(kxw + 9216) = wo[7][1].hi;
  float gb, hx2;
  {
    float a = 0, b = 0;
#pragma unroll
    for (int i = 0; i < 8; ++i) { a += nrm[(lane & 31) * 8 + i]; b = fmaxf(b, nrm[256 + (lane & 31) * 8 + i]); }
    gb = 0.5f * 1.03f * xsum(sum16(sqrtf(a * b)));
    float s2 = 0, cm = 0;
#pragma unroll
    for (int i = 0; i < 8; ++i) { float sa = mod[lane * 8 + i]; s2 += sa * sa; cm = fmaxf(cm, fabsf(1.f + mod[512 + lane * 8 + i])); }
    float hx = 1.01f * (22.6275f * xmax(max16(cm)) + sqrtf(xsum(sum16(s2))));
    hx2 = hx * hx;
  }
  const int mtok = lane >> 3, msub = lane & 7;
  const lptr mw = lfix(L_M + (2 * w * 64 + msub * 8 + (mtok ^ msub)) * 16);
  const lptr mr = lfix(L_M + ((lane >> 3) * 8 + ((lane & 7) ^ (lane >> 3))) * 16);
  {
    const u16* a = P.mem0 + mtok * 512 + 128 * w + 16 * msub;
#pragma unroll
    for (int f = 0; f < 2; ++f) {
      u32x2 x0 = *(const u32x2*)(a + 4 * f), x1 = *(const u32x2*)(a + 4 * f + 8);
      LWR(mw + f * 1024) = u32x4{x0[0], x1[0], x0[1], x1[1]};
    }
  }
  const u32 kvf = OFF_KVF + h * 256;
  u32x4 ks[2], vx[2];
  wait_ge<2>(P.ws, kvf, 1);
#pragma unroll
  for (int a = 0; a < 2; ++a) { ks[a] = ld4(rw, OFF_KV + h * 16384 + (2 * w + a) * 1024 + lane * 16); vx[a] = ld4(rw, OFF_KV + h * 16384 + 8192 + (2 * w + a) * 1024 + lane * 16); }
  LWR(kxw) = ks[0]; LWR(kxw + 1024) = ks[1];
  u32 fl = ldw(P.ws, kvf + 4), kb0 = OFF_KV + h * 16384 + 2 * w * 1024 + lane * 16, fs = __builtin_amdgcn_readfirstlane(fl);
  const lptr qkw = lfix((w < 2 ? L_Q : L_K) + (8 * (w & 1) + (c >> 1)) * 144 + 64 * (g & 1) + 4 * (c & 1) + 8 * (lane >> 5));
  const lptr qkr = lfix(L_Q + g * 144 + (lane & 7) * 16);
  const lptr wvr = lfix(L_WV + lane * 16);
  const lptr ow = lfix(L_O + ((4 * w + g) * 8 + (lane & 7)) * 16), orr = lfix(L_O + (lane ^ rot) * 16);
  u32 aoff[4];
#pragma unroll
  for (int r = 0; r < 4; ++r) { aoff[r] = (w * 32 + (lane >> 5) * 8 + 4 * (g & 1) + (r ^ rot)) * 256 + c * 8; asm volatile("" : "+v"(aoff[r])); }
  const u32 poff = (w * 32 + mtok) * 256 + msub * 16;
  lds_bar();

  f32x4 aq[4] = {zv(), zv(), zv(), zv()}, av[2] = {zv(), zv()};
  const u32x2 onem = {g < 2 ? 0x3f803f80u : 0u, g < 2 ? 0x3f803f80u : 0u};
  for (int s = 0; s < NSEG - 1; ++s) {
    f32x4 gm, sm, s0, s1;
    u32x4 mf[8];
    u32x8 wb[2][2];
#define LDV(k) _Pragma("unroll") for (int a = 0; a < 2; ++a) { wb[(k) & 1][a].lo = LRD(wvr + (a * 16 + (k) * 2) * 1024); wb[(k) & 1][a].hi = LRD(wvr + (a * 16 + (k) * 2 + 1) * 1024); }
#define MMV(k) { smld(av[0], mf[k], wb[(k) & 1][0], sidx); smld(av[1], mf[k], wb[(k) & 1][1], sidx); }
#define MQK(k) _Pragma("unroll") for (int t = 0; t < 4; ++t) aq[t] = smma(mf[k], wqk[t][k], aq[t], sidx);
#pragma unroll
    for (int k = 0; k < 8; ++k) mf[k] = LRD(mr + k * 1024);
    LDV(0);
    SB;
    if (fs == 0) wait_ge<2>(P.ws, kvf + 4 * (s + 1), 1);
    fl = ldw(P.ws, kvf + 4 * (s + 2));
    SB;
    MQK(0); SB; LDV(1); SB;
    MQK(1); mld0(gm, mf[0], mf[0]); mld(gm, mf[1], mf[1]); SB;
    MQK(2); mld(gm, mf[2], mf[2]); mld(gm, mf[3], mf[3]); mld(gm, mf[4], mf[4]); SB;
    MMV(0); SB; LDV(2); mld(gm, mf[5], mf[5]); mld(gm, mf[6], mf[6]); mld(gm, mf[7], mf[7]); SB;
    MQK(3); SB;
    MMV(1); SB; LDV(3); SB;
    asm volatile("" : "+v"(gm));
    int n2 = max(max(__float_as_int(gm[0]), __float_as_int(gm[1])), max(__float_as_int(gm[2]), __float_as_int(gm[3])));
#define MQ1(k, t) SB; aq[t] = smma(mf[k], wqk[t][k], aq[t], sidx); SB;
    MQ1(4, 0); n2 = imaxd<0xB1>(n2); MQ1(4, 1); n2 = imaxd<0x4E>(n2); MQ1(4, 2); n2 = imaxd<0x141>(n2); MQ1(4, 3); n2 = imaxd<0x128>(n2);
    MQ1(5, 0); auto p16 = __builtin_amdgcn_permlane16_swap((u32)n2, (u32)n2, false, false);
    MQ1(5, 1); n2 = max((int)p16[0], (int)p16[1]);
    MQ1(5, 2); auto p32 = __builtin_amdgcn_permlane32_swap((u32)n2, (u32)n2, false, false);
    MQ1(5, 3); float inv = fmaxf(2.f * __int_as_float(max((int)p32[0], (int)p32[1])), hx2);
    MQ1(6, 0); inv = __builtin_amdgcn_sqrtf(inv);
    MQ1(6, 1); inv = fmaxf(inv * gb, 1e-20f) * (1.f / 2080374784.f);
    MQ1(6, 2); const u32 kb = kb0 + ((s + 1) & 15) * (32 * 16384);
    MQ1(6, 3);
    MQK(7); SB;
    MMV(2); SB;
    LDV(4);
    const u32 e0 = pk(swapadd(aq[0][0], aq[1][0]), swapadd(aq[2][0], aq[3][0])), e1 = pk(swapadd(aq[0][1], aq[1][1]), swapadd(aq[2][1], aq[3][1]));
    const u32 e2 = pk(swapadd(aq[0][2], aq[1][2]), swapadd(aq[2][2], aq[3][2])), e3 = pk(swapadd(aq[0][3], aq[1][3]), swapadd(aq[2][3], aq[3][3]));
    LW1(qkw) = e0; LW1(qkw + 16) = e1; LW1(qkw + 32) = e2; LW1(qkw + 48) = e3;
    SB;
    MMV(3); SB;
    u32x4 qf[4], kf[4], kx[8];
    LDV(5); kx[0] = LRD(kxr); kx[4] = LRD(kxr + 4096);
    SB;
    asm volatile("s_waitcnt lgkmcnt(6)\n\ts_barrier" ::: "memory");
    qf[0] = LRD(qkr); kf[0] = LRD(qkr + 2304); qf[1] = LRD(qkr + 576); kf[1] = LRD(qkr + 2304 + 576);
    SB;
    MMV(4); SB;
    kx[1] = LRD(kxr + 1024); kx[5] = LRD(kxr + 5120); qf[2] = LRD(qkr + 1152); kf[2] = LRD(qkr + 2304 + 1152);
    SB;
    MMV(5); SB;
    kx[2] = LRD(kxr + 2048); kx[6] = LRD(kxr + 6144); qf[3] = LRD(qkr + 1728); kf[3] = LRD(qkr + 2304 + 1728);
    SB;
    mld0(s0, kx[0], qf[0]); mld0(s1, kx[4], qf[0]); mld0(sm, kf[0], qf[0]);
    SB; kx[3] = LRD(kxr + 3072); kx[7] = LRD(kxr + 7168); SB;
    mld(s0, kx[1], qf[1]); mld(s1, kx[5], qf[1]); mld(sm, kf[1], qf[1]);
    mld(s0, kx[2], qf[2]); mld(s1, kx[6], qf[2]); mld(sm, kf[2], qf[2]);
    SB; LDV(6); SB;
    mld(s0, kx[3], qf[3]); mld(s1, kx[7], qf[3]); mld(sm, kf[3], qf[3]);
    SB; LDV(7); SB;
    u32x2 o1, o2;
    asm volatile("v_mov_b64 %0, %5\n\tv_mov_b64 %1, %5\n\ts_nop 1" : "=v"(o1), "=v"(o2), "+v"(s0), "+v"(s1), "+v"(sm) : "s"(0x3f803f803f803f80ull));
    const u32x4 one = {o1[0], o1[1], o2[0], o2[1]};
    float mx = fmax2(fmax3(fmax3(s0[0], s0[1], s0[2]), fmax3(s0[3], s1[0], s1[1]), fmax3(s1[2], s1[3], sm[0])), fmax3(sm[1], sm[2], sm[3]));
    mx = xmax(mx) * SM_C;
    SB; MMV(6); SB;
#pragma unroll
    for (int r = 0; r < 4; ++r) s0[r] = __builtin_amdgcn_exp2f(fmaf(s0[r], SM_C, -mx));
    SB; MMV(7); SB;
#pragma unroll
    for (int r = 0; r < 4; ++r) s1[r] = __builtin_amdgcn_exp2f(fmaf(s1[r], SM_C, -mx));
    u32x4 px = {pk(s0[0], s0[1]), pk(s0[2], s0[3]), pk(s1[0], s1[1]), pk(s1[2], s1[3])};
    f32x4 o[2], rsum;
    SB;
    mvv0(rsum, one, px); mld0(o[0], vx[0], px); mld0(o[1], vx[1], px);
    SB;
#pragma unroll
    for (int a = 0; a < 2; ++a) { ks[a] = ld4(rw, kb + a * 1024); vx[a] = __builtin_amdgcn_raw_buffer_load_b128(rw, kb + a * 1024, 8192, SC1); }
    SB;
    const u32x2 vm0 = {pk(av[0][0], av[0][1]), pk(av[0][2], av[0][3])}, vm1 = {pk(av[1][0], av[1][1]), pk(av[1][2], av[1][3])};
#pragma unroll
    for (int r = 0; r < 4; ++r) sm[r] = __builtin_amdgcn_exp2f(fmaf(sm[r], SM_C, -mx));
    u32x2 pm = {pk(sm[0], sm[1]), pk(sm[2], sm[3])};
    SB;
    m16(rsum, onem, pm);
    asm("v_mfma_f32_16x16x16_bf16 %0, %2, %4, %0\n\tv_mfma_f32_16x16x16_bf16 %1, %3, %4, %1" : "+v"(o[0]), "+v"(o[1]) : "v"(vm0), "v"(vm1), "v"(pm));
    SB;
    asm volatile("s_nop 4" : "+v"(rsum));
    float rs = __builtin_amdgcn_rcpf(rsum[0] * inv);
    asm volatile("s_nop 0" : "+v"(o[0]), "+v"(o[1]), "+v"(rs));
    if (!(lane & 8))
      LWR(ow) = u32x4{pk(o[0][0] * rs, o[1][0] * rs), pk(o[0][1] * rs, o[1][1] * rs), pk(o[0][2] * rs, o[1][2] * rs), pk(o[0][3] * rs, o[1][3] * rs)};
    lds_bar();
    u32x4 of0 = LRD(orr), of1 = LRD(orr + 1024);
    wo[7][1].lo = LRD(kxw + 8192); wo[7][1].hi = LRD(kxw + 9216);
    SB;
    f32x4 t[8] = {zv(), zv(), zv(), zv(), zv(), zv(), zv(), zv()};
    char* ab[2] = {P.ws + OFF_MACC + (s + 1) * 32768, P.ws + OFF_MACC + (s + 1) * 32768 + 4096};
    SB;
#define ENC(q, r) { u32 lo = ((u32)(int)swapadd(t[4 * q][r], t[4 * q + 2][r]) & ~63u) | 1u, hi = (u32)(int)swapadd(t[4 * q + 1][r], t[4 * q + 3][r]); \
      asm volatile("global_atomic_add_x2 %0, %1, %2" :: "v"(aoff[r]), "v"(u32x2{lo, hi}), "s"(ab[q]) : "memory"); }
#pragma unroll
    for (int i = 0; i < 4; ++i) smld(t[i], of0, wo[i][0], sidx);
#pragma unroll
    for (int i = 0; i < 4; ++i) smld(t[i], of1, wo[i][1], sidx);
    SB;
#pragma unroll
    for (int r = 0; r < 4; ++r) {
      smld(t[4 + (2 * r & 3)], r < 2 ? of0 : of1, wo[4 + (2 * r & 3)][r >> 1], sidx);
      smld(t[5 + (2 * r & 3)], r < 2 ? of0 : of1, wo[5 + (2 * r & 3)][r >> 1], sidx);
      if (r == 0) asm volatile("s_nop 5" : "+v"(t[0]), "+v"(t[1]), "+v"(t[2]), "+v"(t[3]));
      SB;
      ENC(0, r);
      SB;
    }
    asm volatile("" : "+v"(t[4]), "+v"(t[5]), "+v"(t[6]), "+v"(t[7]));
#pragma unroll
    for (int r = 0; r < 4; ++r) ENC(1, r);
#pragma unroll
    for (int i = 0; i < 4; ++i) aq[i] = zv();
    av[0] = zv(); av[1] = zv();
    drain();
    fs = __builtin_amdgcn_readfirstlane(fl);
    LWR(kxw) = ks[0]; LWR(kxw + 1024) = ks[1];
#pragma unroll 1
    for (int rounds = 0; rounds < SPIN_CAP; ++rounds) {
      asm volatile("" ::: "memory");
      u32x4 q[4];
#pragma unroll
      for (int i = 0; i < 4; ++i) q[i] = ld4(rw, OFF_MACC + (s + 1) * 32768 + poff + i * 2048);
      SB;
      u32 m = 32;
#pragma unroll
      for (int f = 0; f < 2; ++f) {
        const u32 d0 = dec(q[2 * f][0], q[2 * f][1], inv), d1 = dec(q[2 * f][2], q[2 * f][3], inv);
        SB;
        m &= q[2 * f][0] & q[2 * f][2] & q[2 * f + 1][0] & q[2 * f + 1][2];
        LWR(mw + f * 1024) = u32x4{d0, d1, dec(q[2 * f + 1][0], q[2 * f + 1][1], inv), dec(q[2 * f + 1][2], q[2 * f + 1][3], inv)};
        SB;
      }
      if (__all(m != 0)) break;
    }
    lds_bar();
    if (h == 0 && w == 0 && lane == 0) stw(P.ws, OFF_RDY + (s + 1) * 128, __float_as_uint(inv));
  }
}

DEV void proj_role(const Ptrs& P, char* lds, rsrc_t rw, int h, int r0, int tid, int lane, int w) {
  char* L_H = lds;
  char* L_S = lds + 32768;
  char* L_MT = lds + 81920;
  char* L_KM = lds + 98304;
  const int c = lane & 15, g = lane >> 4;
  u32x4 wq[16][2], wk[16][2], wv[16][2];
  const u32 loff = g * 8 * 4096 + h * 128 + 32 * w + 2 * c;
  load_w2<16, true>(wk, P.wk, 32 * 4096, 4096, loff);
  load_w2<16, true>(wv, P.wv, 32 * 4096, 4096, loff);
#pragma unroll
  for (int i = 0; i < 4; ++i) *(u32x4*)(L_MT + (i * 256 + tid) * 16) = u32x4{0, 0, 0, 0};
  auto kv_part = [&](int s, char* slot) __attribute__((always_inline)) {
    wait_wg<8>(P.ws, OFF_HF + s * 128, 1, w);
    u32x4 t[8];
#pragma unroll
    for (int j = 0; j < 8; ++j) t[j] = ld4(rw, OFF_H + s * 32768 + (j * 256 + tid) * 16);
#pragma unroll
    for (int j = 0; j < 8; ++j) *(u32x4*)(L_H + (j * 256 + tid) * 16) = t[j];
    lds_bar();
    const u32 kb = OFF_KV + ((s & 15) * 32 + h) * 16384 + lane * 16;
    u32x2 vp[2][2];
#pragma unroll
    for (int mt = 0; mt < 2; ++mt) {
      f32x4 ak[2], av[2];
      u32x4 hfa[16];
#pragma unroll
      for (int ks = 0; ks < 16; ++ks) hfa[ks] = *(const u32x4*)(L_H + ((mt * 16 + ks) * 64 + lane) * 16);
      SB;
#pragma unroll
      for (int ks = 0; ks < 16; ++ks) {
        const u32x4 hf = hfa[ks];
#pragma unroll
        for (int a = 0; a < 2; ++a) {
          if (ks == 0) { mav0(ak[a], wk[ks][a], hf); mva0(av[a], hf, wv[ks][a]); } else { mav(ak[a], wk[ks][a], hf); mva(av[a], hf, wv[ks][a]); }
        }
      }
      fin(ak[0], ak[1], av[0], av[1]);
      u32x4 kf = {pk(ak[0][0], ak[0][1]), pk(ak[0][2], ak[0][3]), pk(ak[1][0], ak[1][1]), pk(ak[1][2], ak[1][3])};
      st4(rw, kb + (mt * 4 + w) * 1024, kf);
      *(u32x4*)(slot + 8192 + ((mt * 4 + w) * 64 + lane) * 16) = kf;
#pragma unroll
      for (int a = 0; a < 2; ++a) vp[mt][a] = u32x2{pk(av[a][0], av[a][1]), pk(av[a][2], av[a][3])};
    }
#pragma unroll
    for (int a = 0; a < 2; ++a) {
      u32x4 vf = {vp[0][a][0], vp[0][a][1], vp[1][a][0], vp[1][a][1]};
      st4(rw, kb + 8192 + (2 * w + a) * 1024, vf);
      *(u32x4*)(slot + 16384 + ((2 * w + a) * 64 + lane) * 16) = vf;
    }
  };
  kv_part(r0, L_S);
  drain();
  lds_bar();
  if (tid == 0) stw(P.ws, OFF_KVF + h * 256 + r0 * 4, 1u);
  load_w2<16, false>(wq, P.wq, 32 * 4096, 4096, loff);
  for (int i = 0; i <= 16; ++i) {
    if (i < 16) {
      const int s = 4 * i + r0;
      char* slot = L_S + (i & 1) * 24576;
      if (i > 0) kv_part(s, slot);
#pragma unroll
      for (int mt = 0; mt < 2; ++mt) {
        f32x4 aq[2];
#pragma unroll
        for (int ks = 0; ks < 16; ++ks) {
          u32x4 hf = *(const u32x4*)(L_H + ((mt * 16 + ks) * 64 + lane) * 16);
#pragma unroll
          for (int a = 0; a < 2; ++a) { if (ks == 0) mld0(aq[a], wq[ks][a], hf); else mld(aq[a], wq[ks][a], hf); }
        }
        fin(aq[0], aq[1]);
        *(u32x4*)(slot + ((mt * 4 + w) * 64 + lane) * 16) = u32x4{pk(aq[0][0], aq[0][1]), pk(aq[0][2], aq[0][3]), pk(aq[1][0], aq[1][1]), pk(aq[1][2], aq[1][3])};
      }
      drain();
      lds_bar();
      if (tid == 0) stw(P.ws, OFF_KVF + h * 256 + s * 4, 1u);
    }
    if (i >= 1) {
      const int s = 4 * (i - 1) + r0;
      const char* slot = L_S + ((i - 1) & 1) * 24576 + lane * 16;
      f32x4 x00, x01, x11;
#pragma unroll
      for (int k = 0; k < 4; ++k) {
        u32x4 q0 = *(const u32x4*)(slot + k * 1024), q1 = *(const u32x4*)(slot + (4 + k) * 1024);
        u32x4 k0 = *(const u32x4*)(slot + (8 + k) * 1024), k1 = *(const u32x4*)(slot + (12 + k) * 1024);
        if (k == 0) { mld0(x00, k0, q0); mld0(x01, k0, q1); mld0(x11, k1, q1); } else { mld(x00, k0, q0); mld(x01, k0, q1); mld(x11, k1, q1); }
      }
      fin(x00, x01, x11);
      const int tok = lane >> 3, sub = lane & 7;
      if (s == 0) {
#pragma unroll
        for (int j = 0; j < 2; ++j) {
          const int u = j * 256 + tid, t = u >> 6, cg = u & 63;
          *(u32x4*)(L_MT + ((cg >> 2) * 64 + t + 16 * (cg & 3)) * 16) = *(const u32x4*)(P.mem0 + t * 512 + cg * 8);
        }
      } else {
        wait_wg<BGX>(P.ws, OFF_RDY + s * 128, 1, w, s);
        const float inv = __uint_as_float(ldw(P.ws, OFF_RDY + s * 128));
        u32x4 q[4];
        poll_macc(rw, OFF_MACC + s * 32768 + (w * 32 + tok) * 256 + sub * 16, q);
#pragma unroll
        for (int u = 0; u < 4; ++u)
#pragma unroll
          for (int e = 0; e < 2; ++e)
            *(u32*)(L_MT + ((4 * w + (sub >> 1)) * 64 + tok + 16 * ((2 * sub + e) & 3)) * 16 + 4 * u) = dec(q[u][2 * e], q[u][2 * e + 1], inv);
      }
      lds_bar();
      f32x4 ak[2], av[2];
      u32x4 mfa[16];
#pragma unroll
      for (int ks = 0; ks < 16; ++ks) mfa[ks] = *(const u32x4*)(L_MT + (ks * 64 + lane) * 16);
      SB;
#pragma unroll
      for (int ks = 0; ks < 16; ++ks) {
        const u32x4 mf = mfa[ks];
#pragma unroll
        for (int a = 0; a < 2; ++a) {
          if (ks == 0) { mav0(ak[a], wk[ks][a], mf); mva0(av[a], mf, wv[ks][a]); } else { mav(ak[a], wk[ks][a], mf); mva(av[a], mf, wv[ks][a]); }
        }
      }
      fin(ak[0], ak[1], av[0], av[1]);
      *(u32x4*)(L_KM + (w * 64 + lane) * 16) = u32x4{pk(ak[0][0], ak[0][1]), pk(ak[0][2], ak[0][3]), pk(ak[1][0], ak[1][1]), pk(ak[1][2], ak[1][3])};
      lds_bar();
      f32x4 m0, m1;
#pragma unroll
      for (int k = 0; k < 4; ++k) {
        u32x4 km = *(const u32x4*)(L_KM + (k * 64 + lane) * 16);
        u32x4 q0 = *(const u32x4*)(slot + k * 1024), q1 = *(const u32x4*)(slot + (4 + k) * 1024);
        if (k == 0) { mld0(m0, km, q0); mld0(m1, km, q1); } else { mld(m0, km, q0); mld(m1, km, q1); }
      }
      fin(m0, m1);
      const float NEG = -__builtin_inff();
#pragma unroll
      for (int r = 0; r < 4; ++r) {
        if (g >= 2) { m0[r] = NEG; m1[r] = NEG; }
        if (4 * g + r > c) { x00[r] = NEG; x11[r] = NEG; }
      }
      float a0 = xmax(fmaxf(fmaxf(fmaxf(m0[0], m0[1]), fmaxf(m0[2], m0[3])), fmaxf(fmaxf(x00[0], x00[1]), fmaxf(x00[2], x00[3])))) * SM_C;
      float a1 = xmax(fmaxf(fmaxf(fmaxf(fmaxf(m1[0], m1[1]), fmaxf(m1[2], m1[3])), fmaxf(fmaxf(x01[0], x01[1]), fmaxf(x01[2], x01[3]))),
                            fmaxf(fmaxf(x11[0], x11[1]), fmaxf(x11[2], x11[3])))) * SM_C;
      float t0 = 0, t1 = 0;
#pragma unroll
      for (int r = 0; r < 4; ++r) {
        m0[r] = __builtin_amdgcn_exp2f(fmaf(m0[r], SM_C, -a0)); x00[r] = __builtin_amdgcn_exp2f(fmaf(x00[r], SM_C, -a0));
        m1[r] = __builtin_amdgcn_exp2f(fmaf(m1[r], SM_C, -a1)); x01[r] = __builtin_amdgcn_exp2f(fmaf(x01[r], SM_C, -a1)); x11[r] = __builtin_amdgcn_exp2f(fmaf(x11[r], SM_C, -a1));
        t0 += m0[r] + x00[r]; t1 += m1[r] + x01[r] + x11[r];
      }
      t0 = __builtin_amdgcn_rcpf(xsum(t0)); t1 = __builtin_amdgcn_rcpf(xsum(t1));
      u32x2 pm0 = {pk(m0[0] * t0, m0[1] * t0), pk(m0[2] * t0, m0[3] * t0)}, pm1 = {pk(m1[0] * t1, m1[1] * t1), pk(m1[2] * t1, m1[3] * t1)};
      u32x4 px0 = {pk(x00[0] * t0, x00[1] * t0), pk(x00[2] * t0, x00[3] * t0), 0u, 0u};
      u32x4 px1 = {pk(x01[0] * t1, x01[1] * t1), pk(x01[2] * t1, x01[3] * t1), pk(x11[0] * t1, x11[1] * t1), pk(x11[2] * t1, x11[3] * t1)};
      f32x4 o0[2], o1[2];
#pragma unroll
      for (int a = 0; a < 2; ++a) {
        u32x4 vf = *(const u32x4*)(slot + (16 + 2 * w + a) * 1024);
        mvv0(o0[a], vf, px0); mvv0(o1[a], vf, px1);
      }
      fin(o0[0], o0[1], o1[0], o1[1]);
#pragma unroll
      for (int a = 0; a < 2; ++a) {
        u32x2 vm = {pk(av[a][0], av[a][1]), pk(av[a][2], av[a][3])};
        m16(o0[a], vm, pm0); m16(o1[a], vm, pm1);
      }
      fin(o0[0], o0[1], o1[0], o1[1]);
      const u32 ob = OFF_O + (s * 256 + 4 * h + w) * 1024 + lane * 16;
      st4(rw, ob, u32x4{pk(o0[0][0], o0[1][0]), pk(o0[0][1], o0[1][1]), pk(o0[0][2], o0[1][2]), pk(o0[0][3], o0[1][3])});
      st4(rw, ob + 131072, u32x4{pk(o1[0][0], o1[1][0]), pk(o1[0][1], o1[1][1]), pk(o1[0][2], o1[1][2]), pk(o1[0][3], o1[1][3])});
      drain();
      lds_bar();
      if (tid == 0) addw(P.ws, OFF_OC + s * 128);
    }
  }
}

DEV void oproj_role(const Ptrs& P, char* lds, rsrc_t rw, int n, int th, int lane, int w) {
  const int c = lane & 15, g = lane >> 4;
  const float* mod = (const float*)(P.ws + OFF_MOD);
  u32x4 wo[32][2];
  load_w2<32, true>(wo, P.wo, 32 * 512, 512, (w * 1024 + g * 8) * 512 + 32 * n + 2 * c);
  float ga[8];
#pragma unroll
  for (int j = 0; j < 8; ++j) ga[j] = mod[1024 + 32 * n + 8 * g + j];
  for (int s = 0; s < NSEG; ++s) {
    const u32 row = (s * 32 + th * 16 + c) * 512 + 32 * n + 8 * g;
    u32x4 xr = *(const u32x4*)(P.x + row);
    wait_wg<BG>(P.ws, OFF_OC + s * 128, 32, w, s);
    u32x4 of[32];
#pragma unroll
    for (int k = 0; k < 32; ++k) of[k] = ld4(rw, OFF_O + ((s * 2 + th) * 128 + 32 * w + k) * 1024 + lane * 16);
    SB;
    f32x4 a0, a1;
#pragma unroll
    for (int k = 0; k < 32; ++k) {
      if (k == 0) { mav0(a0, wo[k][0], of[k]); mav0(a1, wo[k][1], of[k]); } else { mav(a0, wo[k][0], of[k]); mav(a1, wo[k][1], of[k]); }
    }
    fin(a0, a1);
    char* red = lds + (s & 1) * 8192;
    *(f32x4*)(red + (w * 128 + lane) * 16) = a0; *(f32x4*)(red + (w * 128 + 64 + lane) * 16) = a1;
    lds_bar();
    if (w == (s & 3)) {
      a0 = f32x4{0, 0, 0, 0}; a1 = a0;
#pragma unroll
      for (int i = 0; i < 4; ++i) { a0 += *(const f32x4*)(red + (i * 128 + lane) * 16); a1 += *(const f32x4*)(red + (i * 128 + 64 + lane) * 16); }
      u32x4 y;
#pragma unroll
      for (int r = 0; r < 4; ++r) y[r] = pk(bflo(xr[r]) + rbf(ga[2 * r] * rbf(a0[r])), bfhi(xr[r]) + rbf(ga[2 * r + 1] * rbf(a1[r])));
      st4(rw, OFF_X1 + row * 2, y);
      drain();
      if (lane == 0) addw(P.ws, OFF_X1C + (s * 2 + th) * 128);
    }
  }
}

template <int NCH, int LPR> DEV void ln_coef(u32x4 (&sh)[NCH], u32x4 (&sc)[NCH], const float* shift, const float* scl, int part) {
#pragma unroll
  for (int i = 0; i < NCH; ++i)
#pragma unroll
    for (int p = 0; p < 4; ++p) {
      const int d = (i * LPR + part) * 8 + 2 * p;
      const f32x2 a = *(const f32x2*)(shift + d), b = *(const f32x2*)(scl + d);
      sh[i][p] = pk(a[0], a[1]); sc[i][p] = pk(1.f + b[0], 1.f + b[1]);
    }
}
template <int NCH, int LPR> DEV void ln_mod(u32x4 (&v)[NCH], const u32x4 (&sh)[NCH], const u32x4 (&sc)[NCH]) {
  float sum = 0;
#pragma unroll
  for (int i = 0; i < NCH; ++i)
#pragma unroll
    for (int p = 0; p < 4; ++p) sum += bflo(v[i][p]) + bfhi(v[i][p]);
  const float mean = (LPR == 8 ? sum8(sum) : sum16(sum)) * (1.f / 512.f);
  float var = 0;
#pragma unroll
  for (int i = 0; i < NCH; ++i)
#pragma unroll
    for (int p = 0; p < 4; ++p) { float a = bflo(v[i][p]) - mean, b = bfhi(v[i][p]) - mean; var += a * a + b * b; }
  const float rstd = 1.f / sqrtf((LPR == 8 ? sum8(var) : sum16(var)) * (1.f / 512.f) + 1e-5f);
#pragma unroll
  for (int i = 0; i < NCH; ++i)
#pragma unroll
    for (int p = 0; p < 4; ++p)
      v[i][p] = pk(fmaf((bflo(v[i][p]) - mean) * rstd, bflo(sc[i][p]), bflo(sh[i][p])), fmaf((bfhi(v[i][p]) - mean) * rstd, bfhi(sc[i][p]), bfhi(sh[i][p])));
}

DEV void ln1_task(const Ptrs& P, rsrc_t rw, int s, int tid) {
  const float* mod = (const float*)(P.ws + OFF_MOD);
  const int tok = tid >> 3, part = tid & 7;
  u32x4 v[8];
#pragma unroll
  for (int i = 0; i < 8; ++i) v[i] = *(const u32x4*)(P.x + (s * 32 + tok) * 512 + (i * 8 + part) * 8);
  u32x4 sh[8], sc[8];
  ln_coef<8, 8>(sh, sc, mod, mod + 512, part);
  ln_mod<8, 8>(v, sh, sc);
#pragma unroll
  for (int i = 0; i < 8; ++i) st4(rw, OFF_H + s * 32768 + (((tok >> 4) * 16 + 2 * i + (part >> 2)) * 64 + (tok & 15) + 16 * (part & 3)) * 16, v[i]);
  drain();
  lds_bar();
  if (tid == 0) stw(P.ws, OFF_HF + s * 128, 1u);
}

DEV void ffn1_role(const Ptrs& P, char* lds, rsrc_t rw, int fg, int th, int par, int tid, int lane, int w) {
  const int c = lane & 15, g = lane >> 4, kf = min(4 * fg + w, NKF - 1);
  const bool act = 4 * fg + w < NKF;
  const float* mod = (const float*)(P.ws + OFF_MOD);
  u32x4 w1[16][2];
  load_w2<16, false>(w1, P.w1, 32 * FF, FF, g * 8 * FF + 32 * kf + 2 * c);
  const u32x4 bb = *(const u32x4*)(P.b1 + 32 * kf + 8 * g);
  const int tok = tid >> 4, part = tid & 15;
  u32x4 sh[4], sc[4];
  ln_coef<4, 16>(sh, sc, mod + 1536, mod + 2048, part);
  for (int s = par; s < NSEG; s += 2) {
    wait_wg<BG>(P.ws, OFF_X1C + (s * 2 + th) * 128, 16, w, s);
    u32x4 v[4];
#pragma unroll
    for (int i = 0; i < 4; ++i) v[i] = ld4(rw, OFF_X1 + ((s * 32 + th * 16 + tok) * 512 + (i * 16 + part) * 8) * 2);
    ln_mod<4, 16>(v, sh, sc);
    char* lh = lds;
#pragma unroll
    for (int i = 0; i < 4; ++i) *(u32x4*)(lh + ((4 * i + (part >> 2)) * 64 + (tok ^ (part >> 2)) + 16 * (part & 3)) * 16) = v[i];
    lds_bar();
    f32x4 a0 = {}, a1 = {};
    u32x4 hf[16];
#pragma unroll
    for (int k = 0; k < 16; ++k) hf[k] = *(const u32x4*)(lh + (k * 64 + (lane ^ (k & 3))) * 16);
    SB;
#pragma unroll
    for (int k = 0; k < 16; ++k) { a0 = mma(w1[k][0], hf[k], a0); a1 = mma(w1[k][1], hf[k], a1); }
    u32x4 y;
#pragma unroll
    for (int r = 0; r < 4; ++r) {
      float u[2] = {rbf(rbf(a0[r]) + bflo(bb[r])), rbf(rbf(a1[r]) + bfhi(bb[r]))};
#pragma unroll
      for (int e = 0; e < 2; ++e) {
        float z = 1.5957691216057308f * (u[e] + 0.044715f * u[e] * u[e] * u[e]);
        u[e] = u[e] * __builtin_amdgcn_rcpf(1.f + __expf(-z));
      }
      y[r] = pk(u[0], u[1]);
    }
    if (act) st4(rw, OFF_G + ((s * 2 + th) * NKF + kf) * 1024 + lane * 16, y);
    drain();
    lds_bar();
    if (tid == 0) addw(P.ws, OFF_GC + (s * 2 + th) * 128);
  }
}

DEV void ffn2_role(const Ptrs& P, char* lds, rsrc_t rw, int n, int th, int lane, int w) {
  const int c = lane & 15, g = lane >> 4;
  const float* mod = (const float*)(P.ws + OFF_MOD);
  u32x4 w2[11][4];
#pragma unroll
  for (int i = 0; i < 11; ++i) {
    const int kf = min(w + 4 * i, NKF - 1);
    u32x2 t[8];
#pragma unroll
    for (int j = 0; j < 8; ++j) t[j] = *(const u32x2*)(P.w2 + (size_t)(32 * kf + 8 * g + j) * 512 + 64 * n + 4 * c);
#pragma unroll
    for (int p = 0; p < 4; ++p) {
      w2[i][0][p] = ilo(t[2 * p][0], t[2 * p + 1][0]); w2[i][1][p] = ihi(t[2 * p][0], t[2 * p + 1][0]);
      w2[i][2][p] = ilo(t[2 * p][1], t[2 * p + 1][1]); w2[i][3][p] = ihi(t[2 * p][1], t[2 * p + 1][1]);
    }
  }
  const int col = 64 * n + 16 * g + 4 * w;
  const f32x4 gbv = *(const f32x4*)(mod + 2560 + col);
  const u32x2 b2 = *(const u32x2*)(P.b2 + col);
  for (int s = 0; s < NSEG; ++s) {
    wait_wg<BG>(P.ws, OFF_GC + (s * 2 + th) * 128, 11, w, s);
    u32x4 gf[11];
#pragma unroll
    for (int i = 0; i < 11; ++i) gf[i] = ld4(rw, OFF_G + ((s * 2 + th) * NKF + min(w + 4 * i, NKF - 1)) * 1024 + lane * 16);
    const u32 row = (s * 32 + th * 16 + c) * 512 + col;
    const u32x2 x1 = __builtin_amdgcn_raw_buffer_load_b64(rw, OFF_X1 + row * 2, 0, SC1);
    SB;
    if (w == 3) gf[10] = u32x4{0, 0, 0, 0};
    f32x4 acc[4] = {};
#pragma unroll
    for (int i = 0; i < 11; ++i)
#pragma unroll
      for (int t = 0; t < 4; ++t) acc[t] = mma(w2[i][t], gf[i], acc[t]);
    char* red = lds + (s & 1) * 16384;
#pragma unroll
    for (int r = 0; r < 4; ++r) *(f32x4*)(red + ((w * 4 + r) * 64 + lane) * 16) = f32x4{acc[0][r], acc[1][r], acc[2][r], acc[3][r]};
    lds_bar();
    f32x4 y = {};
#pragma unroll
    for (int i = 0; i < 4; ++i) y += *(const f32x4*)(red + ((i * 4 + w) * 64 + lane) * 16);
    u32x2 o;
#pragma unroll
    for (int p = 0; p < 2; ++p)
      o[p] = pk(bflo(x1[p]) + rbf(gbv[2 * p] * rbf(rbf(y[2 * p]) + bflo(b2[p]))), bfhi(x1[p]) + rbf(gbv[2 * p + 1] * rbf(rbf(y[2 * p + 1]) + bfhi(b2[p]))));
    *(u32x2*)(P.out + row) = o;
  }
}

__global__ __launch_bounds__(256, 1) void main_kernel(const u16* x, const u16* mem0, const u16* wq, const u16* wk, const u16* wv, const u16* wo,
                                                      const u16* w1, const u16* b1, const u16* w2, const u16* b2, u16* out, char* ws) {
  __shared__ __attribute__((aligned(16))) char lds[LDS_BYTES];
  const Ptrs P = {x, mem0, wq, wk, wv, wo, w1, b1, w2, b2, out, ws};
  const int b = blockIdx.x, tid = threadIdx.x, lane = tid & 63, w = __builtin_amdgcn_readfirstlane(tid >> 6);
  const rsrc_t rw = __builtin_amdgcn_make_buffer_rsrc(ws, 0, WS_BYTES, 0x00020000);
  if (b < 32) {
    chain_role(P, lds, rw, b, lane, w);
  } else if (b < 160) {
    proj_role(P, lds, rw, (b - 32) & 31, (b - 32) >> 5, tid, lane, w);
  } else if (b < 192) {
    for (int i = 0; i < 8; ++i) __builtin_amdgcn_s_sleep(36);
    oproj_role(P, lds, rw, (b - 160) >> 1, b & 1, lane, w);
  } else {
    ln1_task(P, rw, b - 192, tid);
    for (int i = 0; i < 8; ++i) __builtin_amdgcn_s_sleep(18);
    if (b < 208) ffn2_role(P, lds, rw, (b - 192) >> 1, b & 1, lane, w);
    else if (b < 252) ffn1_role(P, lds, rw, ((b - 208) % 22) >> 1, b & 1, (b - 208) / 22, tid, lane, w);
  }
}

extern "C" void kernel_launch(void* const* d_in, const int* in_sizes, int n_in, void* d_out, int out_size, void* d_ws, size_t ws_size,
                              hipStream_t stream) {
  const u16* const* in = (const u16* const*)d_in;
  setup_kernel<<<304, 256, 0, stream>>>(in[12], in[13], in[2], in[3], in[4], in[5], (char*)d_ws);
  main_kernel<<<256, 256, 0, stream>>>(in[0], in[1], in[2], in[3], in[4], in[5], in[8], in[9], in[10], in[11], (u16*)d_out, (char*)d_ws);
}
```
